```python
import torch

def _softmax(x, axis=-1):
    return torch.softmax(x, dim=axis)
def _gelu(x):
    return torch.nn.functional.gelu(x, approximate="tanh")
def _silu(x):
    return torch.nn.functional.silu(x)
def _sigmoid(x):
    return torch.sigmoid(x)
def _layernorm(x, axis=-1, eps=1e-5):
    m = x.mean(dim=axis, keepdim=True)
    v = x.var(dim=axis, keepdim=True, unbiased=False)
    return (x - m) / torch.sqrt(v + eps)
def _scatter_add(tgt, idx, src, axis=0):
    return tgt.clone().index_add_(axis, idx, src)
def _segment_sum(src, seg, n):
    out = torch.zeros((n,) + src.shape[1:], dtype=src.dtype)
    return out.index_add_(0, seg, src)
def _pad(x, pw):
    flat = tuple(v for pair in reversed(pw) for v in pair)
    return torch.nn.functional.pad(x, flat)

kernel_name = 'concat_heads__mha_block_cached__compressed_kv_cla__decode'

def setup_inputs():

    _w = (lambda a: (lambda t: t.to(torch.bfloat16) if t.is_floating_point() else t)(torch.as_tensor(a)))
    def _A_setup():
        B, Sq, D = 1, 1, 640
        H, Sk, Dh = 32, 8192, 192
        return {
            "x": _w((lambda *s: torch.randn(*s, dtype=torch.bfloat16) * 1.6)(B, Sq, D)),
            "k_cache": _w((lambda *s: torch.randn(*s, dtype=torch.bfloat16) * 1.6)(B, H, Sk, Dh)),
            "v_cache": _w((lambda *s: torch.randn(*s, dtype=torch.bfloat16) * 0.05)(B, H, Sk, Dh)),
            "wq": _w((lambda *s: torch.randn(*s, dtype=torch.bfloat16) * 1.6)(D, H * Dh) * (D ** -0.5)),
            "wo": _w((lambda *s: torch.randn(*s, dtype=torch.bfloat16) * 0.05)(H * Dh, D) * ((H * Dh) ** -0.5)),
        }
    _A_in = _A_setup()

    def _B_setup():
        B, H, Sq, Sk, D, M = 1, 32, 1, 8192, 192, 640
        Dc = 4 * D
        return {
            "x": _w((lambda *s: torch.randn(*s, dtype=torch.bfloat16) * 3.2)(B, Sq, M)),
            "c_kv": _w((lambda *s: torch.randn(*s, dtype=torch.bfloat16) * 0.1)(B, Sk, Dc)),
            "w_q": _w((lambda *s: torch.randn(*s, dtype=torch.bfloat16) * 6.4)(M, H, D) * (M ** -0.5)),
            "w_uk": _w((lambda *s: torch.randn(*s, dtype=torch.bfloat16) * 6.4)(Dc, H, D) * (Dc ** -0.5)),
            "w_uv": _w((lambda *s: torch.randn(*s, dtype=torch.bfloat16) * 0.1)(Dc, H, D) * (Dc ** -0.5)),
            "w_o": _w((lambda *s: torch.randn(*s, dtype=torch.bfloat16) * 0.1)(H, D, M) * ((H * D) ** -0.5)),
        }
    _B_in = _B_setup()

    return {
        "A_x": _A_in["x"],
        "A_k_cache": _A_in["k_cache"],
        "A_v_cache": _A_in["v_cache"],
        "A_wq": _A_in["wq"],
        "A_wo": _A_in["wo"],
        "B_x": _B_in["x"],
        "B_c_kv": _B_in["c_kv"],
        "B_w_q": _B_in["w_q"],
        "B_w_uk": _B_in["w_uk"],
        "B_w_uv": _B_in["w_uv"],
        "B_w_o": _B_in["w_o"],
    }

def reference(A_x, A_k_cache, A_v_cache, A_wq, A_wo, B_x, B_c_kv, B_w_q, B_w_uk, B_w_uv, B_w_o):
    def _A_ref(x, k_cache, v_cache, wq, wo):
        B, Sq, D = x.shape
        H, Dh = 32, 192
        q = (lambda x, *d: x.permute(*d))((x @ wq).reshape(B, Sq, H, Dh), 0, 2, 1, 3)
        scores = torch.einsum("bhqd,bhkd->bhqk", q, k_cache) * (Dh ** -0.5)
        p = _softmax(scores, axis=-1)
        o = torch.einsum("bhqk,bhkd->bhqd", p, v_cache)
        return (lambda x, *d: x.permute(*d))(o, 0, 2, 1, 3).reshape(B, Sq, H * Dh) @ wo

    def _B_ref(x, c_kv, w_q, w_uk, w_uv, w_o):
        B, Sq, M = x.shape[0], x.shape[1], x.shape[2]
        Sk = c_kv.shape[1]
        H, D = w_q.shape[1], w_q.shape[2]
        q = torch.einsum("bqm,mhd->bhqd", x, w_q)
        k = torch.einsum("bkc,chd->bhkd", c_kv, w_uk)
        v = torch.einsum("bkc,chd->bhkd", c_kv, w_uv)
        scores = torch.einsum("bhqd,bhkd->bhqk", q, k) * (D ** -0.5)
        off = Sk - Sq
        qi = torch.arange(Sq)[:, None]
        kj = torch.arange(Sk)[None, :]
        cmask = kj <= (qi + off)
        scores = torch.where(cmask[None, None], scores, -1e9)
        p = _softmax(scores, axis=-1)
        ctx = torch.einsum("bhqk,bhkd->bhqd", p, v)
        return torch.einsum("bhqd,hdm->bqm", ctx, w_o)

    a = _A_ref(A_x, A_k_cache, A_v_cache, A_wq, A_wo)
    b = _B_ref(B_x, B_c_kv, B_w_q, B_w_uk, B_w_uv, B_w_o)
    return torch.concatenate([a, b], axis=-1)
```

```cpp
#include <hip/hip_runtime.h>
#include <hip/hip_bf16.h>
#include <stdint.h>

#define MULTI_LAUNCH 1

#define GAS __attribute__((address_space(1)))
#define LAS __attribute__((address_space(3)))
typedef unsigned u32x4 __attribute__((ext_vector_type(4)));
typedef unsigned u32x2 __attribute__((ext_vector_type(2)));
typedef float f32x4 __attribute__((ext_vector_type(4)));
typedef float f32x2 __attribute__((ext_vector_type(2)));
typedef short s16x8 __attribute__((ext_vector_type(8)));
typedef short s16x4 __attribute__((ext_vector_type(4)));
typedef __bf16 bf16x2_t __attribute__((ext_vector_type(2)));
typedef unsigned short u16;

constexpr int H = 32, SK = 8192, DH = 192, DM = 640, DC = 768, HD = H * DH;
constexpr int NA = 192, NB = 64, NTHR = 512, ASPLIT = 6;
constexpr float SCALE = 0.07216878364870322f;
constexpr float LOG2E = 1.4426950408889634f;

constexpr unsigned CNT_STRIDE = 128;
constexpr unsigned WS_CNT = 0;
constexpr unsigned CNT_BYTES = 64 * CNT_STRIDE;
constexpr int C_HEAD = 0;
constexpr int C_AOUT = 32;
constexpr int C_B1 = 40, C_B2 = 41, C_B3 = 42;
constexpr unsigned WS_APART = 16384;
constexpr unsigned WS_AOUT = WS_APART + NA * 200 * 4;
constexpr unsigned WS_QABS = WS_AOUT + NA * DM * 4;
constexpr unsigned WS_BSTAT = WS_QABS + 2 * H * DC * 2;
constexpr unsigned WS_BOUT = WS_BSTAT + NB * H * 2 * 4;
constexpr unsigned WS_BCTX = WS_BOUT + NB * DM * 4;
constexpr unsigned WS_END = WS_BCTX + NB * H * DC * 4;

constexpr int QROW = 1552;
constexpr int CROW = 784;
constexpr int PROW = 272;
constexpr int L_BIG = 0;
constexpr int L_P = 100352;
constexpr int L_ST = L_P + 32 * PROW;
constexpr int L_RED = L_ST + 2048;
constexpr int L_Q = L_RED + 16384;
constexpr int LDS_BYTES = L_Q + 1024;

struct Params {
  const u16* Ax; const u16* Ak; const u16* Av; const u16* Awq; const u16* Awo;
  const u16* Bx; const u16* Bc; const u16* Bwq; const u16* Bwuk; const u16* Bwuv; const u16* Bwo;
  u16* out; unsigned char* ws;
};

__device__ __forceinline__ float bf_lo(unsigned u) { return __builtin_bit_cast(float, u << 16); }
__device__ __forceinline__ float bf_hi(unsigned u) { return __builtin_bit_cast(float, u & 0xffff0000u); }
__device__ __forceinline__ float bf2f(u16 u) { return __builtin_bit_cast(float, (unsigned)u << 16); }
__device__ __forceinline__ u16 f2bf(float x) { __bf16 b = (__bf16)x; return __builtin_bit_cast(u16, b); }
__device__ __forceinline__ float bf16r(float x) { return bf2f(f2bf(x)); }
__device__ __forceinline__ float dot2(unsigned a, unsigned b, float acc) {
  return __builtin_amdgcn_fdot2_f32_bf16(__builtin_bit_cast(bf16x2_t, a), __builtin_bit_cast(bf16x2_t, b), acc, false);
}
template <int CTRL> __device__ __forceinline__ float dpp(float x) {
  return __builtin_bit_cast(float, __builtin_amdgcn_mov_dpp(__builtin_bit_cast(int, x), CTRL, 0xf, 0xf, true));
}
constexpr int XOR1 = 0xB1, XOR2 = 0x4E, HMIRROR = 0x141, ROR8 = 0x128, ROR4 = 0x124;
__device__ __forceinline__ float sum8(float s) {
  s += dpp<XOR1>(s); s += dpp<XOR2>(s); s += dpp<HMIRROR>(s); return s;
}
__device__ __forceinline__ u32x4 ld16(const u16* p) { return *reinterpret_cast<const u32x4*>(p); }
__device__ __forceinline__ u32x4 ld16nt(const u16* p) { return __builtin_nontemporal_load(reinterpret_cast<const u32x4*>(p)); }

__device__ __forceinline__ __amdgpu_buffer_rsrc_t ws_rsrc(unsigned char* ws) {
  return __builtin_amdgcn_make_buffer_rsrc(ws, 0, (int)WS_END, 0x00020000);
}
__device__ __forceinline__ void st16_dev(__amdgpu_buffer_rsrc_t r, unsigned off, f32x4 v) {
  __builtin_amdgcn_raw_buffer_store_b128(__builtin_bit_cast(u32x4, v), r, off, 0, 16);
}
__device__ __forceinline__ void st16_dev_u(__amdgpu_buffer_rsrc_t r, unsigned off, u32x4 v) {
  __builtin_amdgcn_raw_buffer_store_b128(v, r, off, 0, 16);
}
__device__ __forceinline__ f32x4 ld16_dev(__amdgpu_buffer_rsrc_t r, unsigned off) {
  return __builtin_bit_cast(f32x4, __builtin_amdgcn_raw_buffer_load_b128(r, off, 0, 16));
}
__device__ __forceinline__ u32x4 ld16_dev_u(__amdgpu_buffer_rsrc_t r, unsigned off) {
  return __builtin_amdgcn_raw_buffer_load_b128(r, off, 0, 16);
}
__device__ __forceinline__ void st4_dev(unsigned char* ws, unsigned off, float v) {
  __hip_atomic_store((GAS float*)(ws + off), v, __ATOMIC_RELAXED, __HIP_MEMORY_SCOPE_AGENT);
}
__device__ __forceinline__ float ld4_dev(unsigned char* ws, unsigned off) {
  return __hip_atomic_load((GAS float*)(ws + off), __ATOMIC_RELAXED, __HIP_MEMORY_SCOPE_AGENT);
}
__device__ __forceinline__ void st8_dev(unsigned char* ws, unsigned off, float a, float b) {
  f32x2 v = {a, b};
  __hip_atomic_store((GAS unsigned long long*)(ws + off), __builtin_bit_cast(unsigned long long, v), __ATOMIC_RELAXED, __HIP_MEMORY_SCOPE_AGENT);
}
__device__ __forceinline__ f32x2 ld8_dev(unsigned char* ws, unsigned off) {
  unsigned long long v = __hip_atomic_load((GAS unsigned long long*)(ws + off), __ATOMIC_RELAXED, __HIP_MEMORY_SCOPE_AGENT);
  return __builtin_bit_cast(f32x2, v);
}
__device__ __forceinline__ void publish(unsigned char* ws, int cnt) {
  asm volatile("s_waitcnt vmcnt(0)" ::: "memory");
  __syncthreads();
  if (threadIdx.x == 0)
    __hip_atomic_fetch_add((GAS unsigned*)(ws + WS_CNT + cnt * CNT_STRIDE), 1u, __ATOMIC_RELAXED, __HIP_MEMORY_SCOPE_AGENT);
}
__device__ __forceinline__ void wait_counters(unsigned char* ws, int cnt, int n, unsigned want) {
  __syncthreads();
}

__device__ __forceinline__ void fma_row(float (&acc)[24], float s, const u32x4 (&w)[3]) {
#pragma unroll
  for (int i = 0; i < 3; ++i)
#pragma unroll
    for (int k = 0; k < 4; ++k) {
      acc[i * 8 + 2 * k] = fmaf(s, bf_lo(w[i][k]), acc[i * 8 + 2 * k]);
      acc[i * 8 + 2 * k + 1] = fmaf(s, bf_hi(w[i][k]), acc[i * 8 + 2 * k + 1]);
    }
}
__device__ __forceinline__ float dot_row(const u32x4 (&w)[3], const unsigned (&q)[12]) {
  float s0 = 0.f, s1 = 0.f, s2 = 0.f;
#pragma unroll
  for (int k = 0; k < 4; ++k) { s0 = dot2(w[0][k], q[k], s0); s1 = dot2(w[1][k], q[4 + k], s1); s2 = dot2(w[2][k], q[8 + k], s2); }
  return (s0 + s1) + s2;
}

__device__ __forceinline__ void compute_q(const u16* x, const u16* W, unsigned char* lds, unsigned (&qpk)[12]) {
  const int tid = threadIdx.x, wave = tid >> 6, lane = tid & 63, r = lane >> 3, c = lane & 7;
  float xv[10]; u32x4 w[10][3];
#pragma unroll
  for (int t = 0; t < 10; ++t) xv[t] = bf2f(x[wave * 80 + t * 8 + r]);
#pragma unroll
  for (int t = 0; t < 10; ++t) {
    const u16* p = W + (size_t)(wave * 80 + t * 8 + r) * HD + c * 8;
#pragma unroll
    for (int i = 0; i < 3; ++i) w[t][i] = ld16(p + i * 64);
  }
  float acc[24];
#pragma unroll
  for (int k = 0; k < 24; ++k) acc[k] = 0.f;
#pragma unroll
  for (int t = 0; t < 10; ++t) fma_row(acc, xv[t], w[t]);
#pragma unroll
  for (int k = 0; k < 24; ++k) { float v = acc[k]; v += dpp<ROR8>(v); v += __shfl_xor(v, 16); v += __shfl_xor(v, 32); acc[k] = v; }
  float* red = reinterpret_cast<float*>(lds + L_RED);
  if (r == 0) {
#pragma unroll
    for (int i = 0; i < 3; ++i) {
      *reinterpret_cast<f32x4*>(red + wave * 192 + i * 64 + c * 8) = f32x4{acc[i * 8], acc[i * 8 + 1], acc[i * 8 + 2], acc[i * 8 + 3]};
      *reinterpret_cast<f32x4*>(red + wave * 192 + i * 64 + c * 8 + 4) = f32x4{acc[i * 8 + 4], acc[i * 8 + 5], acc[i * 8 + 6], acc[i * 8 + 7]};
    }
  }
  __syncthreads();
  unsigned* lq = reinterpret_cast<unsigned*>(lds + L_Q);
  if (tid < 96) {
    float a = 0.f, b = 0.f;
#pragma unroll
    for (int w8 = 0; w8 < 8; ++w8) { a += red[w8 * 192 + 2 * tid]; b += red[w8 * 192 + 2 * tid + 1]; }
    lq[tid] = (unsigned)f2bf(a) | ((unsigned)f2bf(b) << 16);
  }
  __syncthreads();
#pragma unroll
  for (int i = 0; i < 3; ++i)
#pragma unroll
    for (int k = 0; k < 4; ++k) qpk[i * 4 + k] = lq[i * 32 + c * 4 + k];
}

__device__ __forceinline__ void a_stream(const Params& p, unsigned char* lds, int wg, int h, int split) {
  const int tid = threadIdx.x, wave = tid >> 6, lane = tid & 63, r = lane >> 3, c = lane & 7;
  const int gw = split * 8 + wave;
  const int t0 = (1024 * gw) / 48, t1 = (1024 * (gw + 1)) / 48, nt = t1 - t0;
  const u16* kb = p.Ak + ((size_t)h * SK + r) * DH + c * 8;
  const u16* vb = p.Av + ((size_t)h * SK + r) * DH + c * 8;
  u32x4 kr[4][3], vr[4][3];
  unsigned qpk[12];
#define A_LOAD(slot, idx) do { const int tt_ = t0 + ((idx) < nt ? (idx) : nt - 1); const size_t o_ = (size_t)tt_ * 8 * DH; \
    _Pragma("unroll") for (int i_ = 0; i_ < 3; ++i_) kr[slot][i_] = ld16nt(kb + o_ + i_ * 64); \
    _Pragma("unroll") for (int i_ = 0; i_ < 3; ++i_) vr[slot][i_] = ld16nt(vb + o_ + i_ * 64); } while (0)
  {
    const u16* x = p.Ax; const u16* W = p.Awq + h * DH;
    float xv[10]; u32x4 w[10][3];
#pragma unroll
    for (int t = 0; t < 10; ++t) xv[t] = bf2f(x[wave * 80 + t * 8 + r]);
#pragma unroll
    for (int t = 0; t < 10; ++t) {
      const u16* q_ = W + (size_t)(wave * 80 + t * 8 + r) * HD + c * 8;
#pragma unroll
      for (int i = 0; i < 3; ++i) w[t][i] = ld16(q_ + i * 64);
    }
    A_LOAD(0, 0); A_LOAD(1, 1);
    float acc[24];
#pragma unroll
    for (int k = 0; k < 24; ++k) acc[k] = 0.f;
#pragma unroll
    for (int t = 0; t < 10; ++t) fma_row(acc, xv[t], w[t]);
    A_LOAD(2, 2); A_LOAD(3, 3);
#pragma unroll
    for (int k = 0; k < 24; ++k) { float v = acc[k]; v += dpp<ROR8>(v); v += __shfl_xor(v, 16); v += __shfl_xor(v, 32); acc[k] = v; }
    float* red = reinterpret_cast<float*>(lds + L_RED);
    if (r == 0) {
#pragma unroll
      for (int i = 0; i < 3; ++i) {
        *reinterpret_cast<f32x4*>(red + wave * 192 + i * 64 + c * 8) = f32x4{acc[i * 8], acc[i * 8 + 1], acc[i * 8 + 2], acc[i * 8 + 3]};
        *reinterpret_cast<f32x4*>(red + wave * 192 + i * 64 + c * 8 + 4) = f32x4{acc[i * 8 + 4], acc[i * 8 + 5], acc[i * 8 + 6], acc[i * 8 + 7]};
      }
    }
    __syncthreads();
    unsigned* lq = reinterpret_cast<unsigned*>(lds + L_Q);
    if (tid < 96) {
      float a = 0.f, b = 0.f;
#pragma unroll
      for (int w8 = 0; w8 < 8; ++w8) { a += red[w8 * 192 + 2 * tid]; b += red[w8 * 192 + 2 * tid + 1]; }
      lq[tid] = (unsigned)f2bf(a) | ((unsigned)f2bf(b) << 16);
    }
    __syncthreads();
#pragma unroll
    for (int i = 0; i < 3; ++i)
#pragma unroll
      for (int k = 0; k < 4; ++k) qpk[i * 4 + k] = lq[i * 32 + c * 4 + k];
  }
  float m = -INFINITY, l = 0.f, acc[24];
#pragma unroll
  for (int k = 0; k < 24; ++k) acc[k] = 0.f;
#define A_STEP(slot, idx) do { \
    float s_ = sum8(dot_row(kr[slot], qpk)); \
    s_ = bf16r(bf16r(s_) * SCALE); \
    s_ = ((idx) < nt) ? s_ : -INFINITY; \
    const float mn_ = fmaxf(m, s_); \
    const float al_ = __builtin_amdgcn_exp2f((m - mn_) * LOG2E), pp_ = __builtin_amdgcn_exp2f((s_ - mn_) * LOG2E); \
    l = fmaf(l, al_, pp_); m = mn_; \
    _Pragma("unroll") for (int k_ = 0; k_ < 24; ++k_) acc[k_] *= al_; \
    fma_row(acc, pp_, vr[slot]); } while (0)
  for (int it = 0; it < 5; ++it) {
#pragma unroll
    for (int d = 0; d < 4; ++d) { A_STEP(d, it * 4 + d); A_LOAD(d, it * 4 + d + 4); }
  }
#pragma unroll
  for (int d = 0; d < 4; ++d) A_STEP(d, 20 + d);
#undef A_STEP
#undef A_LOAD
  float M = m; M = fmaxf(M, dpp<ROR8>(M)); M = fmaxf(M, __shfl_xor(M, 16)); M = fmaxf(M, __shfl_xor(M, 32));
  const float f = __builtin_amdgcn_exp2f((m - M) * LOG2E);
  l *= f; l += dpp<ROR8>(l); l += __shfl_xor(l, 16); l += __shfl_xor(l, 32);
#pragma unroll
  for (int k = 0; k < 24; ++k) { float v = acc[k] * f; v += dpp<ROR8>(v); v += __shfl_xor(v, 16); v += __shfl_xor(v, 32); acc[k] = v; }
  float* red = reinterpret_cast<float*>(lds + L_RED);
  __syncthreads();
  if (r == 0) {
#pragma unroll
    for (int i = 0; i < 3; ++i) {
      *reinterpret_cast<f32x4*>(red + wave * 200 + i * 64 + c * 8) = f32x4{acc[i * 8], acc[i * 8 + 1], acc[i * 8 + 2], acc[i * 8 + 3]};
      *reinterpret_cast<f32x4*>(red + wave * 200 + i * 64 + c * 8 + 4) = f32x4{acc[i * 8 + 4], acc[i * 8 + 5], acc[i * 8 + 6], acc[i * 8 + 7]};
    }
    if (c == 0) { red[wave * 200 + 192] = M; red[wave * 200 + 193] = l; }
  }
  __syncthreads();
  if (tid < 192) {
    float MM = -INFINITY;
#pragma unroll
    for (int w8 = 0; w8 < 8; ++w8) MM = fmaxf(MM, red[w8 * 200 + 192]);
    float o = 0.f, L = 0.f;
#pragma unroll
    for (int w8 = 0; w8 < 8; ++w8) { const float wt = __builtin_amdgcn_exp2f((red[w8 * 200 + 192] - MM) * LOG2E); o = fmaf(wt, red[w8 * 200 + tid], o); L = fmaf(wt, red[w8 * 200 + 193], L); }
    st4_dev(p.ws, WS_APART + (wg * 200 + tid) * 4, o);
    if (tid == 0) st8_dev(p.ws, WS_APART + (wg * 200 + 192) * 4, MM, L);
  }
  publish(p.ws, C_HEAD + h);
}

__device__ __forceinline__ void a_tail(const Params& p, unsigned char* lds, int wg, int h, int split) {
  const int tid = threadIdx.x;
  const int chunk = tid % 80, rg = tid / 80;
  u32x4 w[8];
  if (tid < 320) {
    const u16* wp = p.Awo + (size_t)(h * DH + 32 * split + rg * 8) * DM + chunk * 8;
#pragma unroll
    for (int i = 0; i < 8; ++i) w[i] = ld16(wp + (size_t)i * DM);
  }
  wait_counters(p.ws, C_HEAD + h, 1, ASPLIT);
  float* red = reinterpret_cast<float*>(lds + L_RED);
  float* os = reinterpret_cast<float*>(lds + L_Q);
  if (tid < 192) {
    const int j = tid >> 5, d = tid & 31;
    const int src = h * ASPLIT + j;
    const float o = ld4_dev(p.ws, WS_APART + (src * 200 + 32 * split + d) * 4);
    const f32x2 ml = ld8_dev(p.ws, WS_APART + (src * 200 + 192) * 4);
    red[tid] = o;
    if (d == 0) { red[192 + 2 * j] = ml[0]; red[193 + 2 * j] = ml[1]; }
  }
  __syncthreads();
  if (tid < 32) {
    float MM = -INFINITY;
#pragma unroll
    for (int j = 0; j < ASPLIT; ++j) MM = fmaxf(MM, red[192 + 2 * j]);
    float o = 0.f, L = 0.f;
#pragma unroll
    for (int j = 0; j < ASPLIT; ++j) { const float wt = __builtin_amdgcn_exp2f((red[192 + 2 * j] - MM) * LOG2E); o = fmaf(wt, red[j * 32 + tid], o); L = fmaf(wt, red[193 + 2 * j], L); }
    os[tid] = bf16r(o / L);
  }
  __syncthreads();
  float* red2 = red + 256;
  if (tid < 320) {
    float a[8];
#pragma unroll
    for (int k = 0; k < 8; ++k) a[k] = 0.f;
#pragma unroll
    for (int i = 0; i < 8; ++i) { const float s = os[rg * 8 + i];
#pragma unroll
      for (int k = 0; k < 4; ++k) { a[2 * k] = fmaf(s, bf_lo(w[i][k]), a[2 * k]); a[2 * k + 1] = fmaf(s, bf_hi(w[i][k]), a[2 * k + 1]); } }
    *reinterpret_cast<f32x4*>(red2 + rg * 640 + chunk * 8) = f32x4{a[0], a[1], a[2], a[3]};
    *reinterpret_cast<f32x4*>(red2 + rg * 640 + chunk * 8 + 4) = f32x4{a[4], a[5], a[6], a[7]};
  }
  __syncthreads();
  if (tid < 160) {
    f32x4 s = *reinterpret_cast<f32x4*>(red2 + tid * 4);
#pragma unroll
    for (int g = 1; g < 4; ++g) s += *reinterpret_cast<f32x4*>(red2 + g * 640 + tid * 4);
    st16_dev(ws_rsrc(p.ws), WS_AOUT + (wg * DM + tid * 4) * 4, s);
  }
  publish(p.ws, C_AOUT + (wg & 7));
}

__device__ __forceinline__ void b_qabs(const Params& p, unsigned char* lds, int h, int half) {
  const int tid = threadIdx.x, wave = tid >> 6, lane = tid & 63, r = lane >> 3, c = lane & 7;
  unsigned qpk[12];
  compute_q(p.Bx, p.Bwq + h * DH, lds, qpk);
  u32x4 w[6][3];
#pragma unroll
  for (int t = 0; t < 6; ++t) {
    const u16* q_ = p.Bwuk + (size_t)(384 * half + 48 * wave + 8 * t + r) * HD + h * DH + c * 8;
#pragma unroll
    for (int i = 0; i < 3; ++i) w[t][i] = ld16(q_ + i * 64);
  }
  u16* sh = reinterpret_cast<u16*>(lds + L_RED);
#pragma unroll
  for (int t = 0; t < 6; ++t) {
    const float s = sum8(dot_row(w[t], qpk));
    const u16 hi = f2bf(s); const u16 lo = f2bf(s - bf2f(hi));
    if (c == 0) { sh[48 * wave + 8 * t + r] = hi; sh[384 + 48 * wave + 8 * t + r] = lo; }
  }
  __syncthreads();
  if (tid < 96) {
    const int part = tid / 48, ch = tid % 48;
    const u32x4 v = *reinterpret_cast<const u32x4*>(sh + part * 384 + ch * 8);
    st16_dev_u(ws_rsrc(p.ws), WS_QABS + ((part * H + h) * DC + 384 * half + ch * 8) * 2, v);
  }
  publish(p.ws, C_B1);
}

__device__ __forceinline__ void b_attn(const Params& p, unsigned char* lds, int bb) {
  const int tid = threadIdx.x, wave = tid >> 6, lane = tid & 63, r16 = lane & 15, g = lane >> 4;
  u32x4 cf[24];
  {
    const u16* cp = p.Bc + (size_t)(128 * bb + 16 * wave + r16) * DC + 8 * g;
#pragma unroll
    for (int s = 0; s < 24; ++s) cf[s] = ld16(cp + 32 * s);
  }
  wait_counters(p.ws, C_B1, 1, NB);
  {
    const __amdgpu_buffer_rsrc_t rs = ws_rsrc(p.ws);
#pragma unroll
    for (int i = 0; i < 12; ++i) {
      const int q = tid + 512 * i;
      const int row = q / 96, ch = q % 96;
      const u32x4 v = ld16_dev_u(rs, WS_QABS + q * 16);
      *reinterpret_cast<u32x4*>(lds + L_BIG + row * QROW + ch * 16) = v;
    }
  }
  __syncthreads();
  f32x4 sc[2] = {f32x4{0.f, 0.f, 0.f, 0.f}, f32x4{0.f, 0.f, 0.f, 0.f}};
#pragma unroll
  for (int s = 0; s < 24; ++s) {
#pragma unroll
    for (int ht = 0; ht < 2; ++ht) {
      const s16x8 bh = *reinterpret_cast<const s16x8*>(lds + L_BIG + (16 * ht + r16) * QROW + (32 * s + 8 * g) * 2);
      const s16x8 bl = *reinterpret_cast<const s16x8*>(lds + L_BIG + (32 + 16 * ht + r16) * QROW + (32 * s + 8 * g) * 2);
      sc[ht] = __builtin_amdgcn_mfma_f32_16x16x32_bf16(__builtin_bit_cast(s16x8, cf[s]), bh, sc[ht], 0, 0, 0);
      sc[ht] = __builtin_amdgcn_mfma_f32_16x16x32_bf16(__builtin_bit_cast(s16x8, cf[s]), bl, sc[ht], 0, 0, 0);
    }
  }
  float* wmax = reinterpret_cast<float*>(lds + L_ST);
  float* wsum = wmax + 256;
#pragma unroll
  for (int ht = 0; ht < 2; ++ht) {
#pragma unroll
    for (int k = 0; k < 4; ++k) sc[ht][k] = bf16r(bf16r(sc[ht][k]) * SCALE);
    float mx = fmaxf(fmaxf(sc[ht][0], sc[ht][1]), fmaxf(sc[ht][2], sc[ht][3]));
    mx = fmaxf(mx, __shfl_xor(mx, 16)); mx = fmaxf(mx, __shfl_xor(mx, 32));
    if (g == 0) wmax[wave * 32 + 16 * ht + r16] = mx;
  }
  __syncthreads();
#pragma unroll
  for (int ht = 0; ht < 2; ++ht) {
    float M = -INFINITY;
#pragma unroll
    for (int w8 = 0; w8 < 8; ++w8) M = fmaxf(M, wmax[w8 * 32 + 16 * ht + r16]);
    float e[4], sum = 0.f;
#pragma unroll
    for (int k = 0; k < 4; ++k) { e[k] = __builtin_amdgcn_exp2f((sc[ht][k] - M) * LOG2E); sum += e[k]; }
    sum += __shfl_xor(sum, 16); sum += __shfl_xor(sum, 32);
    if (g == 0) wsum[wave * 32 + 16 * ht + r16] = sum;
    u32x2 pk = {(unsigned)f2bf(e[0]) | ((unsigned)f2bf(e[1]) << 16), (unsigned)f2bf(e[2]) | ((unsigned)f2bf(e[3]) << 16)};
    *reinterpret_cast<u32x2*>(lds + L_P + (16 * ht + r16) * PROW + (16 * wave + 4 * g) * 2) = pk;
  }
#pragma unroll
  for (int s = 0; s < 12; ++s) *reinterpret_cast<u32x4*>(lds + L_BIG + (16 * wave + r16) * CROW + (32 * s + 8 * g) * 2) = cf[s];
  __syncthreads();
  if (tid < 32) {
    float M = -INFINITY, L = 0.f;
#pragma unroll
    for (int w8 = 0; w8 < 8; ++w8) { M = fmaxf(M, wmax[w8 * 32 + tid]); L += wsum[w8 * 32 + tid]; }
    st8_dev(p.ws, WS_BSTAT + (bb * H + tid) * 8, M, L);
  }
  s16x8 pf[2][4];
#pragma unroll
  for (int ht = 0; ht < 2; ++ht)
#pragma unroll
    for (int ks = 0; ks < 4; ++ks) pf[ht][ks] = *reinterpret_cast<const s16x8*>(lds + L_P + (16 * ht + r16) * PROW + (32 * ks + 8 * g) * 2);
  const __amdgpu_buffer_rsrc_t rs = ws_rsrc(p.ws);
  const int q4 = r16 >> 2, p4 = r16 & 3;
#pragma unroll
  for (int ch = 0; ch < 2; ++ch) {
    if (ch == 1) {
      __syncthreads();
#pragma unroll
      for (int s = 0; s < 12; ++s) *reinterpret_cast<u32x4*>(lds + L_BIG + (16 * wave + r16) * CROW + (32 * s + 8 * g) * 2) = cf[12 + s];
      __syncthreads();
    }
#pragma unroll
    for (int ct = 0; ct < 3; ++ct) {
      f32x4 d[2] = {f32x4{0.f, 0.f, 0.f, 0.f}, f32x4{0.f, 0.f, 0.f, 0.f}};
#pragma unroll
      for (int ks = 0; ks < 4; ++ks) {
        const unsigned char* a0 = lds + L_BIG + (32 * ks + 8 * g + q4) * CROW + (48 * wave + 16 * ct + 4 * p4) * 2;
        typedef short v4i16_t __attribute__((ext_vector_type(4)));
        const v4i16_t t0 = __builtin_amdgcn_ds_read_tr16_b64_v4i16((LAS v4i16_t*)(a0));
        const v4i16_t t1 = __builtin_amdgcn_ds_read_tr16_b64_v4i16((LAS v4i16_t*)(a0 + 4 * CROW));
        const s16x8 af = {t0[0], t0[1], t0[2], t0[3], t1[0], t1[1], t1[2], t1[3]};
#pragma unroll
        for (int ht = 0; ht < 2; ++ht) d[ht] = __builtin_amdgcn_mfma_f32_16x16x32_bf16(af, pf[ht][ks], d[ht], 0, 0, 0);
      }
#pragma unroll
      for (int ht = 0; ht < 2; ++ht)
        st16_dev(rs, WS_BCTX + (unsigned)(((bb * H + 16 * ht + r16) * DC + 384 * ch + 48 * wave + 16 * ct + 4 * g) * 4), d[ht]);
    }
  }
  publish(p.ws, C_B2);
}

__device__ __forceinline__ void b_out(const Params& p, unsigned char* lds, int bb, int h, int half) {
  const int tid = threadIdx.x, wave = tid >> 6, lane = tid & 63;
  const int r = lane >> 2, c4 = lane & 3;
  u32x4 w[6][3];
#pragma unroll
  for (int t = 0; t < 6; ++t) {
    const u16* q_ = p.Bwuv + (size_t)(96 * wave + 16 * t + r) * HD + h * DH + 96 * half + c4 * 8;
#pragma unroll
    for (int i = 0; i < 3; ++i) w[t][i] = ld16(q_ + i * 32);
  }
  wait_counters(p.ws, C_B2, 1, NB);
  float* coef = reinterpret_cast<float*>(lds + L_ST);
  float* part = reinterpret_cast<float*>(lds + L_BIG);
  float* red = reinterpret_cast<float*>(lds + L_RED);
  if (tid < 64) {
    const f32x2 ml = ld8_dev(p.ws, WS_BSTAT + (tid * H + h) * 8);
    float M = ml[0];
#pragma unroll
    for (int o = 1; o < 64; o <<= 1) M = fmaxf(M, __shfl_xor(M, o));
    const float wt = __builtin_amdgcn_exp2f((ml[0] - M) * LOG2E);
    float L = wt * ml[1];
#pragma unroll
    for (int o = 1; o < 64; o <<= 1) L += __shfl_xor(L, o);
    coef[tid] = wt / L;
  }
  __syncthreads();
  if (tid < 384) {
    const int jg = tid / 192, q = tid % 192;
    const __amdgpu_buffer_rsrc_t rs = ws_rsrc(p.ws);
    f32x4 a = {0.f, 0.f, 0.f, 0.f};
#pragma unroll
    for (int jb = 0; jb < 2; ++jb) {
      f32x4 v[16];
#pragma unroll
      for (int j = 0; j < 16; ++j) v[j] = ld16_dev(rs, WS_BCTX + (unsigned)((((32 * jg + 16 * jb + j) * H + h) * DC + 4 * q) * 4));
#pragma unroll
      for (int j = 0; j < 16; ++j) a += coef[32 * jg + 16 * jb + j] * v[j];
    }
    *reinterpret_cast<f32x4*>(part + jg * 768 + 4 * q) = a;
  }
  __syncthreads();
  float acc[24];
#pragma unroll
  for (int k = 0; k < 24; ++k) acc[k] = 0.f;
#pragma unroll
  for (int t = 0; t < 6; ++t) { const int row = 96 * wave + 16 * t + r; fma_row(acc, part[row] + part[768 + row], w[t]); }
#pragma unroll
  for (int k = 0; k < 24; ++k) { float v = acc[k]; v += dpp<ROR4>(v); v += dpp<ROR8>(v); v += __shfl_xor(v, 16); v += __shfl_xor(v, 32); acc[k] = v; }
  if (r == 0) {
#pragma unroll
    for (int i = 0; i < 3; ++i) {
      *reinterpret_cast<f32x4*>(red + wave * 96 + i * 32 + c4 * 8) = f32x4{acc[i * 8], acc[i * 8 + 1], acc[i * 8 + 2], acc[i * 8 + 3]};
      *reinterpret_cast<f32x4*>(red + wave * 96 + i * 32 + c4 * 8 + 4) = f32x4{acc[i * 8 + 4], acc[i * 8 + 5], acc[i * 8 + 6], acc[i * 8 + 7]};
    }
  }
  const int chunk = tid % 80, rg = tid / 80;
  u32x4 wo[16];
  if (tid < 480) {
    const u16* wp = p.Bwo + (size_t)(h * DH + 96 * half + rg * 16) * DM + chunk * 8;
#pragma unroll
    for (int i = 0; i < 16; ++i) wo[i] = ld16(wp + (size_t)i * DM);
  }
  __syncthreads();
  float* cs = reinterpret_cast<float*>(lds + L_Q);
  if (tid < 96) {
    float s = 0.f;
#pragma unroll
    for (int w8 = 0; w8 < 8; ++w8) s += red[w8 * 96 + tid];
    cs[tid] = bf16r(s);
  }
  __syncthreads();
  float* red2 = reinterpret_cast<float*>(lds + L_BIG);
  if (tid < 480) {
    float a[8];
#pragma unroll
    for (int k = 0; k < 8; ++k) a[k] = 0.f;
#pragma unroll
    for (int i = 0; i < 16; ++i) { const float s = cs[rg * 16 + i];
#pragma unroll
      for (int k = 0; k < 4; ++k) { a[2 * k] = fmaf(s, bf_lo(wo[i][k]), a[2 * k]); a[2 * k + 1] = fmaf(s, bf_hi(wo[i][k]), a[2 * k + 1]); } }
    *reinterpret_cast<f32x4*>(red2 + rg * 640 + chunk * 8) = f32x4{a[0], a[1], a[2], a[3]};
    *reinterpret_cast<f32x4*>(red2 + rg * 640 + chunk * 8 + 4) = f32x4{a[4], a[5], a[6], a[7]};
  }
  __syncthreads();
  if (tid < 160) {
    f32x4 s = *reinterpret_cast<f32x4*>(red2 + tid * 4);
#pragma unroll
    for (int g = 1; g < 6; ++g) s += *reinterpret_cast<f32x4*>(red2 + g * 640 + tid * 4);
    st16_dev(ws_rsrc(p.ws), WS_BOUT + (bb * DM + tid * 4) * 4, s);
  }
  publish(p.ws, C_B3);
}

__device__ __forceinline__ void final_sums(const Params& p, unsigned char* lds, int bb) {
  const int tid = threadIdx.x;
  const __amdgpu_buffer_rsrc_t rs = ws_rsrc(p.ws);
  float* buf = reinterpret_cast<float*>(lds + L_BIG);
  float* red = reinterpret_cast<float*>(lds + L_RED);
  wait_counters(p.ws, C_B3, 1, NB);
  if (tid < 256) {
    const int j = tid >> 2, q = tid & 3;
    *reinterpret_cast<f32x4*>(buf + j * 16 + q * 4) = ld16_dev(rs, WS_BOUT + (j * DM + 16 * bb + 4 * q) * 4);
  }
  __syncthreads();
  if (tid < 128) {
    const int part = tid >> 4, col = tid & 15; float s = 0.f;
#pragma unroll
    for (int j = 0; j < 8; ++j) s += buf[(part * 8 + j) * 16 + col];
    red[tid] = s;
  }
  __syncthreads();
  if (tid < 16) {
    float s = 0.f;
#pragma unroll
    for (int j = 0; j < 8; ++j) s += red[j * 16 + tid];
    p.out[DM + 16 * bb + tid] = f2bf(s);
  }
  wait_counters(p.ws, C_AOUT, 8, NA / 8);
  if (tid < 384) {
    const int j = tid >> 2, q = tid & 3;
    const f32x4 v0 = ld16_dev(rs, WS_AOUT + (j * DM + 16 * bb + 4 * q) * 4);
    const f32x4 v1 = ld16_dev(rs, WS_AOUT + ((j + 96) * DM + 16 * bb + 4 * q) * 4);
    *reinterpret_cast<f32x4*>(buf + j * 16 + q * 4) = v0;
    *reinterpret_cast<f32x4*>(buf + (j + 96) * 16 + q * 4) = v1;
  }
  __syncthreads();
  if (tid < 128) {
    const int part = tid >> 4, col = tid & 15; float s = 0.f;
#pragma unroll
    for (int j = 0; j < 24; ++j) s += buf[(part * 24 + j) * 16 + col];
    red[tid] = s;
  }
  __syncthreads();
  if (tid < 16) {
    float s = 0.f;
#pragma unroll
    for (int j = 0; j < 8; ++j) s += red[j * 16 + tid];
    p.out[16 * bb + tid] = f2bf(s);
  }
}

__global__ void __launch_bounds__(NTHR, 2) fused_decode_kernel(Params p, int phase) {
  __shared__ __attribute__((aligned(16))) unsigned char lds[LDS_BYTES];
  const int b = blockIdx.x;
  if (b < NA) {
    const int x = b & 7, idx = b >> 3;
    const int h = x * 4 + idx / ASPLIT, split = idx % ASPLIT;
    const int wg = h * ASPLIT + split;
    if (phase < 0 || phase == 0) a_stream(p, lds, wg, h, split);
    if (phase < 0 || phase == 1) a_tail(p, lds, wg, h, split);
  } else {
    const int bb = b - NA;
    const int h = (bb & 7) * 4 + (bb >> 4), half = (bb >> 3) & 1;
    if (phase < 0 || phase == 0) b_qabs(p, lds, h, half);
    if (phase < 0 || phase == 1) b_attn(p, lds, bb);
    if (phase < 0 || phase == 2) b_out(p, lds, bb, h, half);
    if ((phase < 0 || phase == 3) && bb < 40) final_sums(p, lds, bb);
  }
}

extern "C" void kernel_launch(void* const* d_in, const int* in_sizes, int n_in,
                              void* d_out, int out_size, void* d_ws, size_t ws_size,
                              hipStream_t stream) {
  Params p;
  p.Ax = (const u16*)d_in[0]; p.Ak = (const u16*)d_in[1]; p.Av = (const u16*)d_in[2]; p.Awq = (const u16*)d_in[3]; p.Awo = (const u16*)d_in[4];
  p.Bx = (const u16*)d_in[5]; p.Bc = (const u16*)d_in[6]; p.Bwq = (const u16*)d_in[7]; p.Bwuk = (const u16*)d_in[8]; p.Bwuv = (const u16*)d_in[9]; p.Bwo = (const u16*)d_in[10];
  p.out = (u16*)d_out; p.ws = (unsigned char*)d_ws;
  (void)hipMemsetAsync((char*)d_ws + WS_CNT, 0, CNT_BYTES, stream);
  for (int ph = 0; ph < 4; ++ph) fused_decode_kernel<<<NA + NB, NTHR, 0, stream>>>(p, ph);
}
```

```cpp
#include <hip/hip_runtime.h>
#include <hip/hip_bf16.h>
#include <stdint.h>

#define MULTI_LAUNCH 0

#define GAS __attribute__((address_space(1)))
#define LAS __attribute__((address_space(3)))
typedef unsigned u32x4 __attribute__((ext_vector_type(4)));
typedef unsigned u32x2 __attribute__((ext_vector_type(2)));
typedef float f32x4 __attribute__((ext_vector_type(4)));
typedef float f32x2 __attribute__((ext_vector_type(2)));
typedef short s16x8 __attribute__((ext_vector_type(8)));
typedef short s16x4 __attribute__((ext_vector_type(4)));
typedef __bf16 bf16x2_t __attribute__((ext_vector_type(2)));
typedef unsigned short u16;

constexpr int H = 32, SK = 8192, DH = 192, DM = 640, DC = 768, HD = H * DH;
constexpr int NA = 192, NB = 64, NTHR = 512, ASPLIT = 6;
constexpr float SCALE = 0.07216878364870322f;
constexpr float LOG2E = 1.4426950408889634f;

constexpr unsigned CNT_STRIDE = 128;
constexpr unsigned WS_CNT = 0;
constexpr unsigned CNT_BYTES = 64 * CNT_STRIDE;
constexpr int C_HEAD = 0;
constexpr int C_AOUT = 32;
constexpr int C_B1 = 40, C_B2 = 41, C_B3 = 42;
constexpr unsigned WS_APART = 16384;
constexpr unsigned WS_AOUT = WS_APART + NA * 200 * 4;
constexpr unsigned WS_QABS = WS_AOUT + NA * DM * 4;
constexpr unsigned WS_BSTAT = WS_QABS + 2 * H * DC * 2;
constexpr unsigned WS_BOUT = WS_BSTAT + NB * H * 2 * 4;
constexpr unsigned WS_BCTX = WS_BOUT + NB * DM * 4;
constexpr unsigned WS_END = WS_BCTX + NB * H * DC * 4;

constexpr int QROW = 1552;
constexpr int CROW = 784;
constexpr int PROW = 272;
constexpr int L_BIG = 0;
constexpr int L_P = 100352;
constexpr int L_ST = L_P + 32 * PROW;
constexpr int L_RED = L_ST + 2048;
constexpr int L_Q = L_RED + 16384;
constexpr int LDS_BYTES = L_Q + 1024;

struct Params {
  const u16* Ax; const u16* Ak; const u16* Av; const u16* Awq; const u16* Awo;
  const u16* Bx; const u16* Bc; const u16* Bwq; const u16* Bwuk; const u16* Bwuv; const u16* Bwo;
  u16* out; unsigned char* ws;
};

__device__ __forceinline__ float bf_lo(unsigned u) { return __builtin_bit_cast(float, u << 16); }
__device__ __forceinline__ float bf_hi(unsigned u) { return __builtin_bit_cast(float, u & 0xffff0000u); }
__device__ __forceinline__ float bf2f(u16 u) { return __builtin_bit_cast(float, (unsigned)u << 16); }
__device__ __forceinline__ u16 f2bf(float x) { __bf16 b = (__bf16)x; return __builtin_bit_cast(u16, b); }
__device__ __forceinline__ float bf16r(float x) { return bf2f(f2bf(x)); }
__device__ __forceinline__ float dot2(unsigned a, unsigned b, float acc) {
  return __builtin_amdgcn_fdot2_f32_bf16(__builtin_bit_cast(bf16x2_t, a), __builtin_bit_cast(bf16x2_t, b), acc, false);
}
template <int CTRL> __device__ __forceinline__ float dpp(float x) {
  return __builtin_bit_cast(float, __builtin_amdgcn_mov_dpp(__builtin_bit_cast(int, x), CTRL, 0xf, 0xf, true));
}
constexpr int XOR1 = 0xB1, XOR2 = 0x4E, HMIRROR = 0x141, ROR8 = 0x128, ROR4 = 0x124;
__device__ __forceinline__ float sum8(float s) {
  s += dpp<XOR1>(s); s += dpp<XOR2>(s); s += dpp<HMIRROR>(s); return s;
}
__device__ __forceinline__ u32x4 ld16(const u16* p) { return *reinterpret_cast<const u32x4*>(p); }
__device__ __forceinline__ u32x4 ld16nt(const u16* p) { return __builtin_nontemporal_load(reinterpret_cast<const u32x4*>(p)); }

__device__ __forceinline__ __amdgpu_buffer_rsrc_t ws_rsrc(unsigned char* ws) {
  return __builtin_amdgcn_make_buffer_rsrc(ws, 0, (int)WS_END, 0x00020000);
}
__device__ __forceinline__ void st16_dev(__amdgpu_buffer_rsrc_t r, unsigned off, f32x4 v) {
  __builtin_amdgcn_raw_buffer_store_b128(__builtin_bit_cast(u32x4, v), r, off, 0, 16);
}
__device__ __forceinline__ void st16_dev_u(__amdgpu_buffer_rsrc_t r, unsigned off, u32x4 v) {
  __builtin_amdgcn_raw_buffer_store_b128(v, r, off, 0, 16);
}
__device__ __forceinline__ f32x4 ld16_dev(__amdgpu_buffer_rsrc_t r, unsigned off) {
  return __builtin_bit_cast(f32x4, __builtin_amdgcn_raw_buffer_load_b128(r, off, 0, 16));
}
__device__ __forceinline__ u32x4 ld16_dev_u(__amdgpu_buffer_rsrc_t r, unsigned off) {
  return __builtin_amdgcn_raw_buffer_load_b128(r, off, 0, 16);
}
__device__ __forceinline__ void st4_dev(unsigned char* ws, unsigned off, float v) {
  __hip_atomic_store((GAS float*)(ws + off), v, __ATOMIC_RELAXED, __HIP_MEMORY_SCOPE_AGENT);
}
__device__ __forceinline__ float ld4_dev(unsigned char* ws, unsigned off) {
  return __hip_atomic_load((GAS float*)(ws + off), __ATOMIC_RELAXED, __HIP_MEMORY_SCOPE_AGENT);
}
__device__ __forceinline__ void st8_dev(unsigned char* ws, unsigned off, float a, float b) {
  f32x2 v = {a, b};
  __hip_atomic_store((GAS unsigned long long*)(ws + off), __builtin_bit_cast(unsigned long long, v), __ATOMIC_RELAXED, __HIP_MEMORY_SCOPE_AGENT);
}
__device__ __forceinline__ f32x2 ld8_dev(unsigned char* ws, unsigned off) {
  unsigned long long v = __hip_atomic_load((GAS unsigned long long*)(ws + off), __ATOMIC_RELAXED, __HIP_MEMORY_SCOPE_AGENT);
  return __builtin_bit_cast(f32x2, v);
}
__device__ __forceinline__ void publish(unsigned char* ws, int cnt) {
  asm volatile("s_waitcnt vmcnt(0)" ::: "memory");
  __syncthreads();
  if (threadIdx.x == 0)
    __hip_atomic_fetch_add((GAS unsigned*)(ws + WS_CNT + cnt * CNT_STRIDE), 1u, __ATOMIC_RELAXED, __HIP_MEMORY_SCOPE_AGENT);
}
__device__ __forceinline__ void wait_counters(unsigned char* ws, int cnt, int n, unsigned want) {
  if (threadIdx.x < 64) {
    const int lane = threadIdx.x;
    GAS unsigned* p = (GAS unsigned*)(ws + WS_CNT + (cnt + (lane < n ? lane : 0)) * CNT_STRIDE);
    for (unsigned spins = 0; spins < (1u << 22); ++spins) {
      const unsigned v = __hip_atomic_load(p, __ATOMIC_RELAXED, __HIP_MEMORY_SCOPE_AGENT);
      if (__all(v >= want)) break;
      __builtin_amdgcn_s_sleep(2);
    }
  }
  __syncthreads();
}

__device__ __forceinline__ void fma_row(float (&acc)[24], float s, const u32x4 (&w)[3]) {
#pragma unroll
  for (int i = 0; i < 3; ++i)
#pragma unroll
    for (int k = 0; k < 4; ++k) {
      acc[i * 8 + 2 * k] = fmaf(s, bf_lo(w[i][k]), acc[i * 8 + 2 * k]);
      acc[i * 8 + 2 * k + 1] = fmaf(s, bf_hi(w[i][k]), acc[i * 8 + 2 * k + 1]);
    }
}
__device__ __forceinline__ float dot_row(const u32x4 (&w)[3], const unsigned (&q)[12]) {
  float s0 = 0.f, s1 = 0.f, s2 = 0.f;
#pragma unroll
  for (int k = 0; k < 4; ++k) { s0 = dot2(w[0][k], q[k], s0); s1 = dot2(w[1][k], q[4 + k], s1); s2 = dot2(w[2][k], q[8 + k], s2); }
  return (s0 + s1) + s2;
}

__device__ __forceinline__ void compute_q(const u16* x, const u16* W, unsigned char* lds, unsigned (&qpk)[12]) {
  const int tid = threadIdx.x, wave = tid >> 6, lane = tid & 63, r = lane >> 3, c = lane & 7;
  float xv[10]; u32x4 w[10][3];
#pragma unroll
  for (int t = 0; t < 10; ++t) xv[t] = bf2f(x[wave * 80 + t * 8 + r]);
#pragma unroll
  for (int t = 0; t < 10; ++t) {
    const u16* p = W + (size_t)(wave * 80 + t * 8 + r) * HD + c * 8;
#pragma unroll
    for (int i = 0; i < 3; ++i) w[t][i] = ld16(p + i * 64);
  }
  float acc[24];
#pragma unroll
  for (int k = 0; k < 24; ++k) acc[k] = 0.f;
#pragma unroll
  for (int t = 0; t < 10; ++t) fma_row(acc, xv[t], w[t]);
#pragma unroll
  for (int k = 0; k < 24; ++k) { float v = acc[k]; v += dpp<ROR8>(v); v += __shfl_xor(v, 16); v += __shfl_xor(v, 32); acc[k] = v; }
  float* red = reinterpret_cast<float*>(lds + L_RED);
  if (r == 0) {
#pragma unroll
    for (int i = 0; i < 3; ++i) {
      *reinterpret_cast<f32x4*>(red + wave * 192 + i * 64 + c * 8) = f32x4{acc[i * 8], acc[i * 8 + 1], acc[i * 8 + 2], acc[i * 8 + 3]};
      *reinterpret_cast<f32x4*>(red + wave * 192 + i * 64 + c * 8 + 4) = f32x4{acc[i * 8 + 4], acc[i * 8 + 5], acc[i * 8 + 6], acc[i * 8 + 7]};
    }
  }
  __syncthreads();
  unsigned* lq = reinterpret_cast<unsigned*>(lds + L_Q);
  if (tid < 96) {
    float a = 0.f, b = 0.f;
#pragma unroll
    for (int w8 = 0; w8 < 8; ++w8) { a += red[w8 * 192 + 2 * tid]; b += red[w8 * 192 + 2 * tid + 1]; }
    lq[tid] = (unsigned)f2bf(a) | ((unsigned)f2bf(b) << 16);
  }
  __syncthreads();
#pragma unroll
  for (int i = 0; i < 3; ++i)
#pragma unroll
    for (int k = 0; k < 4; ++k) qpk[i * 4 + k] = lq[i * 32 + c * 4 + k];
}

__device__ __forceinline__ void a_stream(const Params& p, unsigned char* lds, int wg, int h, int split) {
  const int tid = threadIdx.x, wave = tid >> 6, lane = tid & 63, r = lane >> 3, c = lane & 7;
  const int gw = split * 8 + wave;
  const int t0 = (1024 * gw) / 48, t1 = (1024 * (gw + 1)) / 48, nt = t1 - t0;
  const u16* kb = p.Ak + ((size_t)h * SK + r) * DH + c * 8;
  const u16* vb = p.Av + ((size_t)h * SK + r) * DH + c * 8;
  u32x4 kr[4][3], vr[4][3];
  unsigned qpk[12];
#define A_LOAD(slot, idx) do { const int tt_ = t0 + ((idx) < nt ? (idx) : nt - 1); const size_t o_ = (size_t)tt_ * 8 * DH; \
    _Pragma("unroll") for (int i_ = 0; i_ < 3; ++i_) kr[slot][i_] = ld16nt(kb + o_ + i_ * 64); \
    _Pragma("unroll") for (int i_ = 0; i_ < 3; ++i_) vr[slot][i_] = ld16nt(vb + o_ + i_ * 64); } while (0)
  {
    const u16* x = p.Ax; const u16* W = p.Awq + h * DH;
    float xv[10]; u32x4 w[10][3];
#pragma unroll
    for (int t = 0; t < 10; ++t) xv[t] = bf2f(x[wave * 80 + t * 8 + r]);
#pragma unroll
    for (int t = 0; t < 10; ++t) {
      const u16* q_ = W + (size_t)(wave * 80 + t * 8 + r) * HD + c * 8;
#pragma unroll
      for (int i = 0; i < 3; ++i) w[t][i] = ld16(q_ + i * 64);
    }
    A_LOAD(0, 0); A_LOAD(1, 1);
    float acc[24];
#pragma unroll
    for (int k = 0; k < 24; ++k) acc[k] = 0.f;
#pragma unroll
    for (int t = 0; t < 10; ++t) fma_row(acc, xv[t], w[t]);
    A_LOAD(2, 2); A_LOAD(3, 3);
#pragma unroll
    for (int k = 0; k < 24; ++k) { float v = acc[k]; v += dpp<ROR8>(v); v += __shfl_xor(v, 16); v += __shfl_xor(v, 32); acc[k] = v; }
    float* red = reinterpret_cast<float*>(lds + L_RED);
    if (r == 0) {
#pragma unroll
      for (int i = 0; i < 3; ++i) {
        *reinterpret_cast<f32x4*>(red + wave * 192 + i * 64 + c * 8) = f32x4{acc[i * 8], acc[i * 8 + 1], acc[i * 8 + 2], acc[i * 8 + 3]};
        *reinterpret_cast<f32x4*>(red + wave * 192 + i * 64 + c * 8 + 4) = f32x4{acc[i * 8 + 4], acc[i * 8 + 5], acc[i * 8 + 6], acc[i * 8 + 7]};
      }
    }
    __syncthreads();
    unsigned* lq = reinterpret_cast<unsigned*>(lds + L_Q);
    if (tid < 96) {
      float a = 0.f, b = 0.f;
#pragma unroll
      for (int w8 = 0; w8 < 8; ++w8) { a += red[w8 * 192 + 2 * tid]; b += red[w8 * 192 + 2 * tid + 1]; }
      lq[tid] = (unsigned)f2bf(a) | ((unsigned)f2bf(b) << 16);
    }
    __syncthreads();
#pragma unroll
    for (int i = 0; i < 3; ++i)
#pragma unroll
      for (int k = 0; k < 4; ++k) qpk[i * 4 + k] = lq[i * 32 + c * 4 + k];
  }
  float m = -INFINITY, l = 0.f, acc[24];
#pragma unroll
  for (int k = 0; k < 24; ++k) acc[k] = 0.f;
#define A_STEP(slot, idx) do { \
    float s_ = sum8(dot_row(kr[slot], qpk)); \
    s_ = bf16r(bf16r(s_) * SCALE); \
    s_ = ((idx) < nt) ? s_ : -INFINITY; \
    const float mn_ = fmaxf(m, s_); \
    const float al_ = __builtin_amdgcn_exp2f((m - mn_) * LOG2E), pp_ = __builtin_amdgcn_exp2f((s_ - mn_) * LOG2E); \
    l = fmaf(l, al_, pp_); m = mn_; \
    _Pragma("unroll") for (int k_ = 0; k_ < 24; ++k_) acc[k_] *= al_; \
    fma_row(acc, pp_, vr[slot]); } while (0)
  for (int it = 0; it < 5; ++it) {
#pragma unroll
    for (int d = 0; d < 4; ++d) { A_STEP(d, it * 4 + d); A_LOAD(d, it * 4 + d + 4); }
  }
#pragma unroll
  for (int d = 0; d < 4; ++d) A_STEP(d, 20 + d);
#undef A_STEP
#undef A_LOAD
  float M = m; M = fmaxf(M, dpp<ROR8>(M)); M = fmaxf(M, __shfl_xor(M, 16)); M = fmaxf(M, __shfl_xor(M, 32));
  const float f = __builtin_amdgcn_exp2f((m - M) * LOG2E);
  l *= f; l += dpp<ROR8>(l); l += __shfl_xor(l, 16); l += __shfl_xor(l, 32);
#pragma unroll
  for (int k = 0; k < 24; ++k) { float v = acc[k] * f; v += dpp<ROR8>(v); v += __shfl_xor(v, 16); v += __shfl_xor(v, 32); acc[k] = v; }
  float* red = reinterpret_cast<float*>(lds + L_RED);
  __syncthreads();
  if (r == 0) {
#pragma unroll
    for (int i = 0; i < 3; ++i) {
      *reinterpret_cast<f32x4*>(red + wave * 200 + i * 64 + c * 8) = f32x4{acc[i * 8], acc[i * 8 + 1], acc[i * 8 + 2], acc[i * 8 + 3]};
      *reinterpret_cast<f32x4*>(red + wave * 200 + i * 64 + c * 8 + 4) = f32x4{acc[i * 8 + 4], acc[i * 8 + 5], acc[i * 8 + 6], acc[i * 8 + 7]};
    }
    if (c == 0) { red[wave * 200 + 192] = M; red[wave * 200 + 193] = l; }
  }
  __syncthreads();
  if (tid < 192) {
    float MM = -INFINITY;
#pragma unroll
    for (int w8 = 0; w8 < 8; ++w8) MM = fmaxf(MM, red[w8 * 200 + 192]);
    float o = 0.f, L = 0.f;
#pragma unroll
    for (int w8 = 0; w8 < 8; ++w8) { const float wt = __builtin_amdgcn_exp2f((red[w8 * 200 + 192] - MM) * LOG2E); o = fmaf(wt, red[w8 * 200 + tid], o); L = fmaf(wt, red[w8 * 200 + 193], L); }
    st4_dev(p.ws, WS_APART + (wg * 200 + tid) * 4, o);
    if (tid == 0) st8_dev(p.ws, WS_APART + (wg * 200 + 192) * 4, MM, L);
  }
  publish(p.ws, C_HEAD + h);
}

__device__ __forceinline__ void a_tail(const Params& p, unsigned char* lds, int wg, int h, int split) {
  const int tid = threadIdx.x;
  const int chunk = tid % 80, rg = tid / 80;
  u32x4 w[8];
  if (tid < 320) {
    const u16* wp = p.Awo + (size_t)(h * DH + 32 * split + rg * 8) * DM + chunk * 8;
#pragma unroll
    for (int i = 0; i < 8; ++i) w[i] = ld16(wp + (size_t)i * DM);
  }
  wait_counters(p.ws, C_HEAD + h, 1, ASPLIT);
  float* red = reinterpret_cast<float*>(lds + L_RED);
  float* os = reinterpret_cast<float*>(lds + L_Q);
  if (tid < 192) {
    const int j = tid >> 5, d = tid & 31;
    const int src = h * ASPLIT + j;
    const float o = ld4_dev(p.ws, WS_APART + (src * 200 + 32 * split + d) * 4);
    const f32x2 ml = ld8_dev(p.ws, WS_APART + (src * 200 + 192) * 4);
    red[tid] = o;
    if (d == 0) { red[192 + 2 * j] = ml[0]; red[193 + 2 * j] = ml[1]; }
  }
  __syncthreads();
  if (tid < 32) {
    float MM = -INFINITY;
#pragma unroll
    for (int j = 0; j < ASPLIT; ++j) MM = fmaxf(MM, red[192 + 2 * j]);
    float o = 0.f, L = 0.f;
#pragma unroll
    for (int j = 0; j < ASPLIT; ++j) { const float wt = __builtin_amdgcn_exp2f((red[192 + 2 * j] - MM) * LOG2E); o = fmaf(wt, red[j * 32 + tid], o); L = fmaf(wt, red[193 + 2 * j], L); }
    os[tid] = bf16r(o / L);
  }
  __syncthreads();
  float* red2 = red + 256;
  if (tid < 320) {
    float a[8];
#pragma unroll
    for (int k = 0; k < 8; ++k) a[k] = 0.f;
#pragma unroll
    for (int i = 0; i < 8; ++i) { const float s = os[rg * 8 + i];
#pragma unroll
      for (int k = 0; k < 4; ++k) { a[2 * k] = fmaf(s, bf_lo(w[i][k]), a[2 * k]); a[2 * k + 1] = fmaf(s, bf_hi(w[i][k]), a[2 * k + 1]); } }
    *reinterpret_cast<f32x4*>(red2 + rg * 640 + chunk * 8) = f32x4{a[0], a[1], a[2], a[3]};
    *reinterpret_cast<f32x4*>(red2 + rg * 640 + chunk * 8 + 4) = f32x4{a[4], a[5], a[6], a[7]};
  }
  __syncthreads();
  if (tid < 160) {
    f32x4 s = *reinterpret_cast<f32x4*>(red2 + tid * 4);
#pragma unroll
    for (int g = 1; g < 4; ++g) s += *reinterpret_cast<f32x4*>(red2 + g * 640 + tid * 4);
    st16_dev(ws_rsrc(p.ws), WS_AOUT + (wg * DM + tid * 4) * 4, s);
  }
  publish(p.ws, C_AOUT + (wg & 7));
}

__device__ __forceinline__ void b_qabs(const Params& p, unsigned char* lds, int h, int half) {
  const int tid = threadIdx.x, wave = tid >> 6, lane = tid & 63, r = lane >> 3, c = lane & 7;
  unsigned qpk[12];
  compute_q(p.Bx, p.Bwq + h * DH, lds, qpk);
  u32x4 w[6][3];
#pragma unroll
  for (int t = 0; t < 6; ++t) {
    const u16* q_ = p.Bwuk + (size_t)(384 * half + 48 * wave + 8 * t + r) * HD + h * DH + c * 8;
#pragma unroll
    for (int i = 0; i < 3; ++i) w[t][i] = ld16(q_ + i * 64);
  }
  u16* sh = reinterpret_cast<u16*>(lds + L_RED);
#pragma unroll
  for (int t = 0; t < 6; ++t) {
    const float s = sum8(dot_row(w[t], qpk));
    const u16 hi = f2bf(s); const u16 lo = f2bf(s - bf2f(hi));
    if (c == 0) { sh[48 * wave + 8 * t + r] = hi; sh[384 + 48 * wave + 8 * t + r] = lo; }
  }
  __syncthreads();
  if (tid < 96) {
    const int part = tid / 48, ch = tid % 48;
    const u32x4 v = *reinterpret_cast<const u32x4*>(sh + part * 384 + ch * 8);
    st16_dev_u(ws_rsrc(p.ws), WS_QABS + ((part * H + h) * DC + 384 * half + ch * 8) * 2, v);
  }
  publish(p.ws, C_B1);
}

__device__ __forceinline__ void b_attn(const Params& p, unsigned char* lds, int bb) {
  const int tid = threadIdx.x, wave = tid >> 6, lane = tid & 63, r16 = lane & 15, g = lane >> 4;
  u32x4 cf[24];
  {
    const u16* cp = p.Bc + (size_t)(128 * bb + 16 * wave + r16) * DC + 8 * g;
#pragma unroll
    for (int s = 0; s < 24; ++s) cf[s] = ld16(cp + 32 * s);
  }
  wait_counters(p.ws, C_B1, 1, NB);
  {
    const __amdgpu_buffer_rsrc_t rs = ws_rsrc(p.ws);
#pragma unroll
    for (int i = 0; i < 12; ++i) {
      const int q = tid + 512 * i;
      const int row = q / 96, ch = q % 96;
      const u32x4 v = ld16_dev_u(rs, WS_QABS + q * 16);
      *reinterpret_cast<u32x4*>(lds + L_BIG + row * QROW + ch * 16) = v;
    }
  }
  __syncthreads();
  f32x4 sc[2] = {f32x4{0.f, 0.f, 0.f, 0.f}, f32x4{0.f, 0.f, 0.f, 0.f}};
#pragma unroll
  for (int s = 0; s < 24; ++s) {
#pragma unroll
    for (int ht = 0; ht < 2; ++ht) {
      const s16x8 bh = *reinterpret_cast<const s16x8*>(lds + L_BIG + (16 * ht + r16) * QROW + (32 * s + 8 * g) * 2);
      const s16x8 bl = *reinterpret_cast<const s16x8*>(lds + L_BIG + (32 + 16 * ht + r16) * QROW + (32 * s + 8 * g) * 2);
      sc[ht] = __builtin_amdgcn_mfma_f32_16x16x32_bf16(__builtin_bit_cast(s16x8, cf[s]), bh, sc[ht], 0, 0, 0);
      sc[ht] = __builtin_amdgcn_mfma_f32_16x16x32_bf16(__builtin_bit_cast(s16x8, cf[s]), bl, sc[ht], 0, 0, 0);
    }
  }
  float* wmax = reinterpret_cast<float*>(lds + L_ST);
  float* wsum = wmax + 256;
#pragma unroll
  for (int ht = 0; ht < 2; ++ht) {
#pragma unroll
    for (int k = 0; k < 4; ++k) sc[ht][k] = bf16r(bf16r(sc[ht][k]) * SCALE);
    float mx = fmaxf(fmaxf(sc[ht][0], sc[ht][1]), fmaxf(sc[ht][2], sc[ht][3]));
    mx = fmaxf(mx, __shfl_xor(mx, 16)); mx = fmaxf(mx, __shfl_xor(mx, 32));
    if (g == 0) wmax[wave * 32 + 16 * ht + r16] = mx;
  }
  __syncthreads();
#pragma unroll
  for (int ht = 0; ht < 2; ++ht) {
    float M = -INFINITY;
#pragma unroll
    for (int w8 = 0; w8 < 8; ++w8) M = fmaxf(M, wmax[w8 * 32 + 16 * ht + r16]);
    float e[4], sum = 0.f;
#pragma unroll
    for (int k = 0; k < 4; ++k) { e[k] = __builtin_amdgcn_exp2f((sc[ht][k] - M) * LOG2E); sum += e[k]; }
    sum += __shfl_xor(sum, 16); sum += __shfl_xor(sum, 32);
    if (g == 0) wsum[wave * 32 + 16 * ht + r16] = sum;
    u32x2 pk = {(unsigned)f2bf(e[0]) | ((unsigned)f2bf(e[1]) << 16), (unsigned)f2bf(e[2]) | ((unsigned)f2bf(e[3]) << 16)};
    *reinterpret_cast<u32x2*>(lds + L_P + (16 * ht + r16) * PROW + (16 * wave + 4 * g) * 2) = pk;
  }
#pragma unroll
  for (int s = 0; s < 12; ++s) *reinterpret_cast<u32x4*>(lds + L_BIG + (16 * wave + r16) * CROW + (32 * s + 8 * g) * 2) = cf[s];
  __syncthreads();
  if (tid < 32) {
    float M = -INFINITY, L = 0.f;
#pragma unroll
    for (int w8 = 0; w8 < 8; ++w8) { M = fmaxf(M, wmax[w8 * 32 + tid]); L += wsum[w8 * 32 + tid]; }
    st8_dev(p.ws, WS_BSTAT + (bb * H + tid) * 8, M, L);
  }
  s16x8 pf[2][4];
#pragma unroll
  for (int ht = 0; ht < 2; ++ht)
#pragma unroll
    for (int ks = 0; ks < 4; ++ks) pf[ht][ks] = *reinterpret_cast<const s16x8*>(lds + L_P + (16 * ht + r16) * PROW + (32 * ks + 8 * g) * 2);
  const __amdgpu_buffer_rsrc_t rs = ws_rsrc(p.ws);
  const int q4 = r16 >> 2, p4 = r16 & 3;
#pragma unroll
  for (int ch = 0; ch < 2; ++ch) {
    if (ch == 1) {
      __syncthreads();
#pragma unroll
      for (int s = 0; s < 12; ++s) *reinterpret_cast<u32x4*>(lds + L_BIG + (16 * wave + r16) * CROW + (32 * s + 8 * g) * 2) = cf[12 + s];
      __syncthreads();
    }
#pragma unroll
    for (int ct = 0; ct < 3; ++ct) {
      f32x4 d[2] = {f32x4{0.f, 0.f, 0.f, 0.f}, f32x4{0.f, 0.f, 0.f, 0.f}};
#pragma unroll
      for (int ks = 0; ks < 4; ++ks) {
        const unsigned char* a0 = lds + L_BIG + (32 * ks + 8 * g + q4) * CROW + (48 * wave + 16 * ct + 4 * p4) * 2;
        typedef short v4i16_t __attribute__((ext_vector_type(4)));
        const v4i16_t t0 = __builtin_amdgcn_ds_read_tr16_b64_v4i16((LAS v4i16_t*)(a0));
        const v4i16_t t1 = __builtin_amdgcn_ds_read_tr16_b64_v4i16((LAS v4i16_t*)(a0 + 4 * CROW));
        const s16x8 af = {t0[0], t0[1], t0[2], t0[3], t1[0], t1[1], t1[2], t1[3]};
#pragma unroll
        for (int ht = 0; ht < 2; ++ht) d[ht] = __builtin_amdgcn_mfma_f32_16x16x32_bf16(af, pf[ht][ks], d[ht], 0, 0, 0);
      }
#pragma unroll
      for (int ht = 0; ht < 2; ++ht)
        st16_dev(rs, WS_BCTX + (unsigned)(((bb * H + 16 * ht + r16) * DC + 384 * ch + 48 * wave + 16 * ct + 4 * g) * 4), d[ht]);
    }
  }
  publish(p.ws, C_B2);
}

__device__ __forceinline__ void b_out(const Params& p, unsigned char* lds, int bb, int h, int half) {
  const int tid = threadIdx.x, wave = tid >> 6, lane = tid & 63;
  const int r = lane >> 2, c4 = lane & 3;
  u32x4 w[6][3];
#pragma unroll
  for (int t = 0; t < 6; ++t) {
    const u16* q_ = p.Bwuv + (size_t)(96 * wave + 16 * t + r) * HD + h * DH + 96 * half + c4 * 8;
#pragma unroll
    for (int i = 0; i < 3; ++i) w[t][i] = ld16(q_ + i * 32);
  }
  wait_counters(p.ws, C_B2, 1, NB);
  float* coef = reinterpret_cast<float*>(lds + L_ST);
  float* part = reinterpret_cast<float*>(lds + L_BIG);
  float* red = reinterpret_cast<float*>(lds + L_RED);
  if (tid < 64) {
    const f32x2 ml = ld8_dev(p.ws, WS_BSTAT + (tid * H + h) * 8);
    float M = ml[0];
#pragma unroll
    for (int o = 1; o < 64; o <<= 1) M = fmaxf(M, __shfl_xor(M, o));
    const float wt = __builtin_amdgcn_exp2f((ml[0] - M) * LOG2E);
    float L = wt * ml[1];
#pragma unroll
    for (int o = 1; o < 64; o <<= 1) L += __shfl_xor(L, o);
    coef[tid] = wt / L;
  }
  __syncthreads();
  if (tid < 384) {
    const int jg = tid / 192, q = tid % 192;
    const __amdgpu_buffer_rsrc_t rs = ws_rsrc(p.ws);
    f32x4 a = {0.f, 0.f, 0.f, 0.f};
#pragma unroll
    for (int jb = 0; jb < 2; ++jb) {
      f32x4 v[16];
#pragma unroll
      for (int j = 0; j < 16; ++j) v[j] = ld16_dev(rs, WS_BCTX + (unsigned)((((32 * jg + 16 * jb + j) * H + h) * DC + 4 * q) * 4));
#pragma unroll
      for (int j = 0; j < 16; ++j) a += coef[32 * jg + 16 * jb + j] * v[j];
    }
    *reinterpret_cast<f32x4*>(part + jg * 768 + 4 * q) = a;
  }
  __syncthreads();
  float acc[24];
#pragma unroll
  for (int k = 0; k < 24; ++k) acc[k] = 0.f;
#pragma unroll
  for (int t = 0; t < 6; ++t) { const int row = 96 * wave + 16 * t + r; fma_row(acc, part[row] + part[768 + row], w[t]); }
#pragma unroll
  for (int k = 0; k < 24; ++k) { float v = acc[k]; v += dpp<ROR4>(v); v += dpp<ROR8>(v); v += __shfl_xor(v, 16); v += __shfl_xor(v, 32); acc[k] = v; }
  if (r == 0) {
#pragma unroll
    for (int i = 0; i < 3; ++i) {
      *reinterpret_cast<f32x4*>(red + wave * 96 + i * 32 + c4 * 8) = f32x4{acc[i * 8], acc[i * 8 + 1], acc[i * 8 + 2], acc[i * 8 + 3]};
      *reinterpret_cast<f32x4*>(red + wave * 96 + i * 32 + c4 * 8 + 4) = f32x4{acc[i * 8 + 4], acc[i * 8 + 5], acc[i * 8 + 6], acc[i * 8 + 7]};
    }
  }
  const int chunk = tid % 80, rg = tid / 80;
  u32x4 wo[16];
  if (tid < 480) {
    const u16* wp = p.Bwo + (size_t)(h * DH + 96 * half + rg * 16) * DM + chunk * 8;
#pragma unroll
    for (int i = 0; i < 16; ++i) wo[i] = ld16(wp + (size_t)i * DM);
  }
  __syncthreads();
  float* cs = reinterpret_cast<float*>(lds + L_Q);
  if (tid < 96) {
    float s = 0.f;
#pragma unroll
    for (int w8 = 0; w8 < 8; ++w8) s += red[w8 * 96 + tid];
    cs[tid] = bf16r(s);
  }
  __syncthreads();
  float* red2 = reinterpret_cast<float*>(lds + L_BIG);
  if (tid < 480) {
    float a[8];
#pragma unroll
    for (int k = 0; k < 8; ++k) a[k] = 0.f;
#pragma unroll
    for (int i = 0; i < 16; ++i) { const float s = cs[rg * 16 + i];
#pragma unroll
      for (int k = 0; k < 4; ++k) { a[2 * k] = fmaf(s, bf_lo(wo[i][k]), a[2 * k]); a[2 * k + 1] = fmaf(s, bf_hi(wo[i][k]), a[2 * k + 1]); } }
    *reinterpret_cast<f32x4*>(red2 + rg * 640 + chunk * 8) = f32x4{a[0], a[1], a[2], a[3]};
    *reinterpret_cast<f32x4*>(red2 + rg * 640 + chunk * 8 + 4) = f32x4{a[4], a[5], a[6], a[7]};
  }
  __syncthreads();
  if (tid < 160) {
    f32x4 s = *reinterpret_cast<f32x4*>(red2 + tid * 4);
#pragma unroll
    for (int g = 1; g < 6; ++g) s += *reinterpret_cast<f32x4*>(red2 + g * 640 + tid * 4);
    st16_dev(ws_rsrc(p.ws), WS_BOUT + (bb * DM + tid * 4) * 4, s);
  }
  publish(p.ws, C_B3);
}

__device__ __forceinline__ void final_sums(const Params& p, unsigned char* lds, int bb) {
  const int tid = threadIdx.x;
  const __amdgpu_buffer_rsrc_t rs = ws_rsrc(p.ws);
  float* buf = reinterpret_cast<float*>(lds + L_BIG);
  float* red = reinterpret_cast<float*>(lds + L_RED);
  wait_counters(p.ws, C_B3, 1, NB);
  if (tid < 256) {
    const int j = tid >> 2, q = tid & 3;
    *reinterpret_cast<f32x4*>(buf + j * 16 + q * 4) = ld16_dev(rs, WS_BOUT + (j * DM + 16 * bb + 4 * q) * 4);
  }
  __syncthreads();
  if (tid < 128) {
    const int part = tid >> 4, col = tid & 15; float s = 0.f;
#pragma unroll
    for (int j = 0; j < 8; ++j) s += buf[(part * 8 + j) * 16 + col];
    red[tid] = s;
  }
  __syncthreads();
  if (tid < 16) {
    float s = 0.f;
#pragma unroll
    for (int j = 0; j < 8; ++j) s += red[j * 16 + tid];
    p.out[DM + 16 * bb + tid] = f2bf(s);
  }
  wait_counters(p.ws, C_AOUT, 8, NA / 8);
  if (tid < 384) {
    const int j = tid >> 2, q = tid & 3;
    const f32x4 v0 = ld16_dev(rs, WS_AOUT + (j * DM + 16 * bb + 4 * q) * 4);
    const f32x4 v1 = ld16_dev(rs, WS_AOUT + ((j + 96) * DM + 16 * bb + 4 * q) * 4);
    *reinterpret_cast<f32x4*>(buf + j * 16 + q * 4) = v0;
    *reinterpret_cast<f32x4*>(buf + (j + 96) * 16 + q * 4) = v1;
  }
  __syncthreads();
  if (tid < 128) {
    const int part = tid >> 4, col = tid & 15; float s = 0.f;
#pragma unroll
    for (int j = 0; j < 24; ++j) s += buf[(part * 24 + j) * 16 + col];
    red[tid] = s;
  }
  __syncthreads();
  if (tid < 16) {
    float s = 0.f;
#pragma unroll
    for (int j = 0; j < 8; ++j) s += red[j * 16 + tid];
    p.out[16 * bb + tid] = f2bf(s);
  }
}

__global__ void __launch_bounds__(NTHR, 2) fused_decode_kernel(Params p, int phase) {
  __shared__ __attribute__((aligned(16))) unsigned char lds[LDS_BYTES];
  const int b = blockIdx.x;
  if (b < NA) {
    const int x = b & 7, idx = b >> 3;
    const int h = x * 4 + idx / ASPLIT, split = idx % ASPLIT;
    const int wg = h * ASPLIT + split;
    if (phase < 0 || phase == 0) a_stream(p, lds, wg, h, split);
    if (phase < 0 || phase == 1) a_tail(p, lds, wg, h, split);
  } else {
    const int bb = b - NA;
    const int h = (bb & 7) * 4 + (bb >> 4), half = (bb >> 3) & 1;
    if (phase < 0 || phase == 0) b_qabs(p, lds, h, half);
    if (phase < 0 || phase == 1) b_attn(p, lds, bb);
    if (phase < 0 || phase == 2) b_out(p, lds, bb, h, half);
    if ((phase < 0 || phase == 3) && bb < 40) final_sums(p, lds, bb);
  }
}

extern "C" void kernel_launch(void* const* d_in, const int* in_sizes, int n_in,
                              void* d_out, int out_size, void* d_ws, size_t ws_size,
                              hipStream_t stream) {
  Params p;
  p.Ax = (const u16*)d_in[0]; p.Ak = (const u16*)d_in[1]; p.Av = (const u16*)d_in[2]; p.Awq = (const u16*)d_in[3]; p.Awo = (const u16*)d_in[4];
  p.Bx = (const u16*)d_in[5]; p.Bc = (const u16*)d_in[6]; p.Bwq = (const u16*)d_in[7]; p.Bwuk = (const u16*)d_in[8]; p.Bwuv = (const u16*)d_in[9]; p.Bwo = (const u16*)d_in[10];
  p.out = (u16*)d_out; p.ws = (unsigned char*)d_ws;
  (void)hipMemsetAsync((char*)d_ws + WS_CNT, 0, CNT_BYTES, stream);
  fused_decode_kernel<<<NA + NB, NTHR, 0, stream>>>(p, -1);
}
```

```cpp
#include <hip/hip_runtime.h>
#include <hip/hip_bf16.h>
#include <stdint.h>

#define GAS __attribute__((address_space(1)))
#define LAS __attribute__((address_space(3)))
typedef unsigned u32x4 __attribute__((ext_vector_type(4)));
typedef unsigned u32x2 __attribute__((ext_vector_type(2)));
typedef float f32x4 __attribute__((ext_vector_type(4)));
typedef float f32x2 __attribute__((ext_vector_type(2)));
typedef short s16x8 __attribute__((ext_vector_type(8)));
typedef short s16x4 __attribute__((ext_vector_type(4)));
typedef __bf16 bf16x2_t __attribute__((ext_vector_type(2)));
typedef unsigned short u16;

constexpr int H = 32, SK = 8192, DH = 192, DM = 640, DC = 768, HD = H * DH;
constexpr int NA = 192, NB = 64, NTHR = 512, ASPLIT = 6;
constexpr float SCALE = 0.07216878364870322f;
constexpr float LOG2E = 1.4426950408889634f;

constexpr unsigned CNT_STRIDE = 128;
constexpr unsigned WS_CNT = 0;
constexpr unsigned WS_QG = 48 * CNT_STRIDE;
constexpr unsigned WS_XG = WS_QG + 2 * 3072 * 8;
constexpr unsigned CNT_BYTES = WS_XG + H * 2 * DH * 8;
constexpr int C_HEAD = 0;
constexpr int C_AOUT = 32;
constexpr int C_B1 = 40, C_B2 = 41;
constexpr unsigned WS_APART = 196608;
constexpr unsigned WS_AOUT = WS_APART + NA * 200 * 4;
constexpr unsigned WS_QABS = WS_AOUT + NA * 2 * DM * 4;
constexpr unsigned WS_BSTAT = WS_QABS + 2 * H * DC * 2;
constexpr unsigned WS_BCTX = WS_BSTAT + NB * H * 2 * 4;
constexpr unsigned WS_END = WS_BCTX + NB * H * DC * 4;
static_assert(CNT_BYTES <= WS_APART && CNT_BYTES % 16 == 0, "ws");

constexpr int QROW = 1552;
constexpr int CROW = 784;
constexpr int PROW = 272;
constexpr int L_BIG = 0;
constexpr int L_P = 100352;
constexpr int L_ST = L_P + 32 * PROW;
constexpr int L_RED = L_ST + 2048;
constexpr int L_Q = L_RED + 16384;
constexpr int LDS_BYTES = L_Q + 1024;
constexpr int LA_WO = 0;
constexpr int LA_WOB = 40960;
constexpr int LA_RED = 81920;
constexpr int LA_Q = LA_RED + 16384;
static_assert(LA_Q + 4096 <= LDS_BYTES, "lds");

#define STAMP(i) do { } while (0)
struct Params {
  const u16* Ax; const u16* Ak; const u16* Av; const u16* Awq; const u16* Awo;
  const u16* Bx; const u16* Bc; const u16* Bwq; const u16* Bwuk; const u16* Bwuv; const u16* Bwo;
  u16* out; unsigned char* ws;
};

__device__ __forceinline__ float bf_lo(unsigned u) { return __builtin_bit_cast(float, u << 16); }
__device__ __forceinline__ float bf_hi(unsigned u) { return __builtin_bit_cast(float, u & 0xffff0000u); }
__device__ __forceinline__ float bf2f(u16 u) { return __builtin_bit_cast(float, (unsigned)u << 16); }
__device__ __forceinline__ u16 f2bf(float x) { __bf16 b = (__bf16)x; return __builtin_bit_cast(u16, b); }
__device__ __forceinline__ float bf16r(float x) { return bf2f(f2bf(x)); }
__device__ __forceinline__ float dot2(unsigned a, unsigned b, float acc) {
  return __builtin_amdgcn_fdot2_f32_bf16(__builtin_bit_cast(bf16x2_t, a), __builtin_bit_cast(bf16x2_t, b), acc, false);
}
template <int CTRL> __device__ __forceinline__ float dpp(float x) {
  return __builtin_bit_cast(float, __builtin_amdgcn_mov_dpp(__builtin_bit_cast(int, x), CTRL, 0xf, 0xf, true));
}
constexpr int XOR1 = 0xB1, XOR2 = 0x4E, HMIRROR = 0x141, ROR8 = 0x128, ROR4 = 0x124;
__device__ __forceinline__ float sum8(float s) {
  s += dpp<XOR1>(s); s += dpp<XOR2>(s); s += dpp<HMIRROR>(s); return s;
}
__device__ __forceinline__ u32x4 ld16(const u16* p) { return __builtin_nontemporal_load(reinterpret_cast<const u32x4*>(p)); }

__device__ __forceinline__ __amdgpu_buffer_rsrc_t ws_rsrc(unsigned char* ws) {
  return __builtin_amdgcn_make_buffer_rsrc(ws, 0, (int)WS_END, 0x00020000);
}
__device__ __forceinline__ void st16_dev(__amdgpu_buffer_rsrc_t r, unsigned off, f32x4 v) {
  __builtin_amdgcn_raw_buffer_store_b128(__builtin_bit_cast(u32x4, v), r, off, 0, 16);
}
__device__ __forceinline__ void st16_dev_u(__amdgpu_buffer_rsrc_t r, unsigned off, u32x4 v) {
  __builtin_amdgcn_raw_buffer_store_b128(v, r, off, 0, 16);
}
__device__ __forceinline__ f32x4 ld16_dev(__amdgpu_buffer_rsrc_t r, unsigned off) {
  return __builtin_bit_cast(f32x4, __builtin_amdgcn_raw_buffer_load_b128(r, off, 0, 16));
}
__device__ __forceinline__ u32x4 ld16_dev_u(__amdgpu_buffer_rsrc_t r, unsigned off) {
  return __builtin_amdgcn_raw_buffer_load_b128(r, off, 0, 16);
}
__device__ __forceinline__ void st4_dev(unsigned char* ws, unsigned off, float v) {
  __hip_atomic_store((GAS float*)(ws + off), v, __ATOMIC_RELAXED, __HIP_MEMORY_SCOPE_AGENT);
}
__device__ __forceinline__ void st4_dev_u(unsigned char* ws, unsigned off, unsigned v) {
  __hip_atomic_store((GAS unsigned*)(ws + off), v, __ATOMIC_RELAXED, __HIP_MEMORY_SCOPE_AGENT);
}
__device__ __forceinline__ float ld4_dev(unsigned char* ws, unsigned off) {
  return __hip_atomic_load((GAS float*)(ws + off), __ATOMIC_RELAXED, __HIP_MEMORY_SCOPE_AGENT);
}
__device__ __forceinline__ void st8_dev(unsigned char* ws, unsigned off, float a, float b) {
  f32x2 v = {a, b};
  __hip_atomic_store((GAS unsigned long long*)(ws + off), __builtin_bit_cast(unsigned long long, v), __ATOMIC_RELAXED, __HIP_MEMORY_SCOPE_AGENT);
}
__device__ __forceinline__ f32x2 ld8_dev(unsigned char* ws, unsigned off) {
  unsigned long long v = __hip_atomic_load((GAS unsigned long long*)(ws + off), __ATOMIC_RELAXED, __HIP_MEMORY_SCOPE_AGENT);
  return __builtin_bit_cast(f32x2, v);
}
__device__ __forceinline__ void publish(unsigned char* ws, int cnt) {
  asm volatile("s_waitcnt vmcnt(0)" ::: "memory");
  __syncthreads();
  if (threadIdx.x == 0)
    __hip_atomic_fetch_add((GAS unsigned*)(ws + WS_CNT + cnt * CNT_STRIDE), 1u, __ATOMIC_RELAXED, __HIP_MEMORY_SCOPE_AGENT);
}
__device__ __forceinline__ void wait_counters(unsigned char* ws, int cnt, int n, unsigned want) {
  if (threadIdx.x < 64) {
    const int lane = threadIdx.x;
    GAS unsigned* p = (GAS unsigned*)(ws + WS_CNT + (cnt + (lane < n ? lane : 0)) * CNT_STRIDE);
    for (unsigned spins = 0; spins < (1u << 22); ++spins) {
      const unsigned v = __hip_atomic_load(p, __ATOMIC_RELAXED, __HIP_MEMORY_SCOPE_AGENT);
      if (__all(v >= want)) break;
      __builtin_amdgcn_s_sleep(2);
    }
  }
  __syncthreads();
}

__device__ __forceinline__ void gather_q(unsigned char* ws, int which, int h, unsigned* lq, unsigned (&qpk)[12]) {
  const int tid = threadIdx.x, c = tid & 7;
  if (tid < 64) {
    const int k = tid < 48 ? tid : 0;
    const GAS unsigned long long* g = (const GAS unsigned long long*)(ws + WS_QG + (which * 3072 + h * 96 + 2 * k) * 8);
    unsigned long long a = 0, b = 0;
    for (unsigned spins = 0; spins < (1u << 22); ++spins) {
      a = __hip_atomic_load(g, __ATOMIC_RELAXED, __HIP_MEMORY_SCOPE_AGENT);
      b = __hip_atomic_load(g + 1, __ATOMIC_RELAXED, __HIP_MEMORY_SCOPE_AGENT);
      if (__all((a >> 32) == 1ull && (b >> 32) == 1ull)) break;
      __builtin_amdgcn_s_sleep(2);
    }
    if (tid < 48) { lq[2 * tid] = (unsigned)a; lq[2 * tid + 1] = (unsigned)b; }
  }
  __syncthreads();
#pragma unroll
  for (int i = 0; i < 3; ++i)
#pragma unroll
    for (int k = 0; k < 4; ++k) qpk[i * 4 + k] = lq[i * 32 + c * 4 + k];
}

__device__ __forceinline__ void fma_row(float (&acc)[24], float s, const u32x4 (&w)[3]) {
#pragma unroll
  for (int i = 0; i < 3; ++i)
#pragma unroll
    for (int k = 0; k < 4; ++k) {
      acc[i * 8 + 2 * k] = fmaf(s, bf_lo(w[i][k]), acc[i * 8 + 2 * k]);
      acc[i * 8 + 2 * k + 1] = fmaf(s, bf_hi(w[i][k]), acc[i * 8 + 2 * k + 1]);
    }
}
__device__ __forceinline__ float dot_row(const u32x4 (&w)[3], const unsigned (&q)[12]) {
  float s0 = 0.f, s1 = 0.f, s2 = 0.f;
#pragma unroll
  for (int k = 0; k < 4; ++k) { s0 = dot2(w[0][k], q[k], s0); s1 = dot2(w[1][k], q[4 + k], s1); s2 = dot2(w[2][k], q[8 + k], s2); }
  return (s0 + s1) + s2;
}
__device__ __forceinline__ void proj32(const float* sv, const unsigned char* W, float* red2, __amdgpu_buffer_rsrc_t rs, unsigned dst) {
  const int tid = threadIdx.x, chunk = tid % 80, rg = tid / 80;
  if (tid < 320) {
    float a[8];
#pragma unroll
    for (int k = 0; k < 8; ++k) a[k] = 0.f;
#pragma unroll
    for (int i = 0; i < 8; ++i) {
      const float s = sv[rg * 8 + i];
      const u32x4 w = *reinterpret_cast<const u32x4*>(W + (rg * 8 + i) * 1280 + chunk * 16);
#pragma unroll
      for (int k = 0; k < 4; ++k) { a[2 * k] = fmaf(s, bf_lo(w[k]), a[2 * k]); a[2 * k + 1] = fmaf(s, bf_hi(w[k]), a[2 * k + 1]); }
    }
    *reinterpret_cast<f32x4*>(red2 + rg * 640 + chunk * 8) = f32x4{a[0], a[1], a[2], a[3]};
    *reinterpret_cast<f32x4*>(red2 + rg * 640 + chunk * 8 + 4) = f32x4{a[4], a[5], a[6], a[7]};
  }
  __syncthreads();
  if (tid < 160) {
    f32x4 s = *reinterpret_cast<f32x4*>(red2 + tid * 4);
#pragma unroll
    for (int g = 1; g < 4; ++g) s += *reinterpret_cast<f32x4*>(red2 + g * 640 + tid * 4);
    st16_dev(rs, dst + tid * 16, s);
  }
}

__device__ __forceinline__ void a_role(const Params& p, unsigned char* lds, int wg, int h, int split) {
  const int tid = threadIdx.x, wave = __builtin_amdgcn_readfirstlane(tid >> 6), lane = tid & 63, r = lane >> 3, c = lane & 7;
  const int gw = split * 8 + wave;
  const int t0 = (1024 * gw) / 48, t1 = (1024 * (gw + 1)) / 48, nt = t1 - t0;
  const u16* kb = p.Ak + ((size_t)h * SK + r) * DH + c * 8;
  const u16* vb = p.Av + ((size_t)h * SK + r) * DH + c * 8;
  const __amdgpu_buffer_rsrc_t rs = ws_rsrc(p.ws);
  u32x4 kr[4][3], vr[4][3];
  unsigned qpk[12];
  float* red = reinterpret_cast<float*>(lds + LA_RED);
#define A_LOAD(slot, idx) do { const int tt_ = t0 + ((idx) < nt ? (idx) : nt - 1); const size_t o_ = (size_t)tt_ * 8 * DH; \
    _Pragma("unroll") for (int i_ = 0; i_ < 3; ++i_) kr[slot][i_] = ld16(kb + o_ + i_ * 64); \
    _Pragma("unroll") for (int i_ = 0; i_ < 3; ++i_) vr[slot][i_] = ld16(vb + o_ + i_ * 64); } while (0)
  {
    const int which = split >= 3 ? 1 : 0, sl = split - 3 * which;
    const u16* x = which ? p.Bx : p.Ax;
    const u16* W = (which ? p.Bwq : p.Awq) + h * DH + 64 * sl + c * 8;
    float xa[10]; u32x4 wa[10];
#pragma unroll
    for (int t = 0; t < 10; ++t) xa[t] = bf2f(x[wave * 80 + t * 8 + r]);
#pragma unroll
    for (int t = 0; t < 10; ++t) wa[t] = ld16(W + (size_t)(wave * 80 + t * 8 + r) * HD);
#pragma unroll
    for (int d = 0; d < 4; ++d) A_LOAD(d, d);
    float a[8];
#pragma unroll
    for (int k = 0; k < 8; ++k) a[k] = 0.f;
#pragma unroll
    for (int t = 0; t < 10; ++t)
#pragma unroll
      for (int k = 0; k < 4; ++k) { a[2 * k] = fmaf(xa[t], bf_lo(wa[t][k]), a[2 * k]); a[2 * k + 1] = fmaf(xa[t], bf_hi(wa[t][k]), a[2 * k + 1]); }
#pragma unroll
    for (int k = 0; k < 8; ++k) { float v = a[k]; v += dpp<ROR8>(v); v += __shfl_xor(v, 16); v += __shfl_xor(v, 32); a[k] = v; }
    if (r == 0) {
      *reinterpret_cast<f32x4*>(red + wave * 64 + c * 8) = f32x4{a[0], a[1], a[2], a[3]};
      *reinterpret_cast<f32x4*>(red + wave * 64 + c * 8 + 4) = f32x4{a[4], a[5], a[6], a[7]};
    }
    __syncthreads();
    if (tid < 32) {
      float s0 = 0.f, s1 = 0.f;
#pragma unroll
      for (int w8 = 0; w8 < 8; ++w8) { s0 += red[w8 * 64 + 2 * tid]; s1 += red[w8 * 64 + 2 * tid + 1]; }
      const unsigned long long gv = (1ull << 32) | (unsigned long long)((unsigned)f2bf(s0) | ((unsigned)f2bf(s1) << 16));
      __hip_atomic_store((GAS unsigned long long*)(p.ws + WS_QG + (which * 3072 + h * 96 + 32 * sl + tid) * 8), gv, __ATOMIC_RELAXED, __HIP_MEMORY_SCOPE_AGENT);
    }
  }
  {
    u32x4 t[10];
    const u16* wo = p.Awo + (size_t)(h * DH + 32 * split) * DM;
    const u16* wob = p.Bwo + (size_t)(h * DH + 32 * split) * DM;
#pragma unroll
    for (int i = 0; i < 5; ++i) { t[i] = ld16(wo + (tid + 512 * i) * 8); t[5 + i] = ld16(wob + (tid + 512 * i) * 8); }
#pragma unroll
    for (int i = 0; i < 5; ++i) {
      *reinterpret_cast<u32x4*>(lds + LA_WO + (tid + 512 * i) * 16) = t[i];
      *reinterpret_cast<u32x4*>(lds + LA_WOB + (tid + 512 * i) * 16) = t[5 + i];
    }
  }
  gather_q(p.ws, 0, h, reinterpret_cast<unsigned*>(lds + LA_Q), qpk);
  STAMP(1);
  float m = -INFINITY, l = 0.f, acc[24];
#pragma unroll
  for (int k = 0; k < 24; ++k) acc[k] = 0.f;
#define A_STEP(slot, idx) do { \
    float s_ = sum8(dot_row(kr[slot], qpk)); \
    s_ = bf16r(bf16r(s_) * SCALE); \
    s_ = ((idx) < nt) ? s_ : -INFINITY; \
    const float mn_ = fmaxf(m, s_); \
    const float al_ = __builtin_amdgcn_exp2f((m - mn_) * LOG2E), pp_ = __builtin_amdgcn_exp2f((s_ - mn_) * LOG2E); \
    l = fmaf(l, al_, pp_); m = mn_; \
    _Pragma("unroll") for (int k_ = 0; k_ < 24; ++k_) acc[k_] *= al_; \
    fma_row(acc, pp_, vr[slot]); } while (0)
  for (int it = 0; it < 5; ++it) {
#pragma unroll
    for (int d = 0; d < 4; ++d) { A_STEP(d, it * 4 + d); A_LOAD(d, it * 4 + d + 4); }
  }
#pragma unroll
  for (int d = 0; d < 4; ++d) A_STEP(d, 20 + d);
#undef A_STEP
#undef A_LOAD
  STAMP(2);
  {
    float M = m; M = fmaxf(M, dpp<ROR8>(M)); M = fmaxf(M, __shfl_xor(M, 16)); M = fmaxf(M, __shfl_xor(M, 32));
    const float f = __builtin_amdgcn_exp2f((m - M) * LOG2E);
    l *= f; l += dpp<ROR8>(l); l += __shfl_xor(l, 16); l += __shfl_xor(l, 32);
#pragma unroll
    for (int k = 0; k < 24; ++k) { float v = acc[k] * f; v += dpp<ROR8>(v); v += __shfl_xor(v, 16); v += __shfl_xor(v, 32); acc[k] = v; }
    if (r == 0) {
#pragma unroll
      for (int i = 0; i < 3; ++i) {
        *reinterpret_cast<f32x4*>(red + wave * 200 + i * 64 + c * 8) = f32x4{acc[i * 8], acc[i * 8 + 1], acc[i * 8 + 2], acc[i * 8 + 3]};
        *reinterpret_cast<f32x4*>(red + wave * 200 + i * 64 + c * 8 + 4) = f32x4{acc[i * 8 + 4], acc[i * 8 + 5], acc[i * 8 + 6], acc[i * 8 + 7]};
      }
      if (c == 0) { red[wave * 200 + 192] = M; red[wave * 200 + 193] = l; }
    }
    __syncthreads();
    if (tid < 192) {
      float MM = -INFINITY;
#pragma unroll
      for (int w8 = 0; w8 < 8; ++w8) MM = fmaxf(MM, red[w8 * 200 + 192]);
      float o = 0.f, L = 0.f;
#pragma unroll
      for (int w8 = 0; w8 < 8; ++w8) { const float wt = __builtin_amdgcn_exp2f((red[w8 * 200 + 192] - MM) * LOG2E); o = fmaf(wt, red[w8 * 200 + tid], o); L = fmaf(wt, red[w8 * 200 + 193], L); }
      st4_dev(p.ws, WS_APART + (wg * 200 + tid) * 4, o);
      if (tid == 0) st8_dev(p.ws, WS_APART + (wg * 200 + 192) * 4, MM, L);
    }
    publish(p.ws, C_HEAD + h);
  }
  STAMP(3);
  float* sv = reinterpret_cast<float*>(lds + LA_Q);
  {
    if (tid < 64) {
      const GAS unsigned long long* g = (const GAS unsigned long long*)(p.ws + WS_XG + ((h * 2 + (tid >> 5)) * DH + 32 * split + (tid & 31)) * 8);
      unsigned long long a = 0;
      for (unsigned spins = 0; spins < (1u << 22); ++spins) {
        a = __hip_atomic_load(g, __ATOMIC_RELAXED, __HIP_MEMORY_SCOPE_AGENT);
        if (__all((a >> 32) == 1ull)) break;
        __builtin_amdgcn_s_sleep(2);
      }
      const float v = __builtin_bit_cast(float, (unsigned)a);
      const float o = __shfl_xor(v, 32);
      if (tid < 32) sv[tid] = bf16r(v + o);
    }
    __syncthreads();
    proj32(sv, lds + LA_WOB, red + 256, rs, WS_AOUT + (wg * 2 * DM + DM) * 4);
  }
  STAMP(4);
  wait_counters(p.ws, C_HEAD + h, 1, ASPLIT);
  STAMP(5);
  if (tid < 192) {
    const int j = tid >> 5, d = tid & 31;
    const int src = h * ASPLIT + j;
    const float o = ld4_dev(p.ws, WS_APART + (src * 200 + 32 * split + d) * 4);
    const f32x2 ml = ld8_dev(p.ws, WS_APART + (src * 200 + 192) * 4);
    red[tid] = o;
    if (d == 0) { red[192 + 2 * j] = ml[0]; red[193 + 2 * j] = ml[1]; }
  }
  __syncthreads();
  if (tid < 32) {
    float MM = -INFINITY;
#pragma unroll
    for (int j = 0; j < ASPLIT; ++j) MM = fmaxf(MM, red[192 + 2 * j]);
    float o = 0.f, L = 0.f;
#pragma unroll
    for (int j = 0; j < ASPLIT; ++j) { const float wt = __builtin_amdgcn_exp2f((red[192 + 2 * j] - MM) * LOG2E); o = fmaf(wt, red[j * 32 + tid], o); L = fmaf(wt, red[193 + 2 * j], L); }
    sv[tid] = bf16r(o / L);
  }
  __syncthreads();
  proj32(sv, lds + LA_WO, red + 256, rs, WS_AOUT + (wg * 2 * DM) * 4);
  publish(p.ws, C_AOUT + (wg & 7));
  STAMP(6);
}

__device__ __forceinline__ void b_role(const Params& p, unsigned char* lds, int bb, int h, int half) {
  const int tid = threadIdx.x, wave = tid >> 6, lane = tid & 63, r = lane >> 3, c = lane & 7, r16 = lane & 15, g = lane >> 4;
  const __amdgpu_buffer_rsrc_t rs = ws_rsrc(p.ws);
  u32x4 cf[24];
  {
    u32x4 w[6][3];
#pragma unroll
    for (int t = 0; t < 6; ++t) {
      const u16* q_ = p.Bwuk + (size_t)(384 * half + 48 * wave + 8 * t + r) * HD + h * DH + c * 8;
#pragma unroll
      for (int i = 0; i < 3; ++i) w[t][i] = ld16(q_ + i * 64);
    }
    unsigned qpk[12];
    gather_q(p.ws, 1, h, reinterpret_cast<unsigned*>(lds + L_Q), qpk);
    u16* sh = reinterpret_cast<u16*>(lds + L_RED);
#pragma unroll
    for (int t = 0; t < 6; ++t) {
      const float s = sum8(dot_row(w[t], qpk));
      const u16 hi = f2bf(s); const u16 lo = f2bf(s - bf2f(hi));
      if (c == 0) { sh[48 * wave + 8 * t + r] = hi; sh[384 + 48 * wave + 8 * t + r] = lo; }
    }
    __syncthreads();
    if (tid < 96) {
      const int part = tid / 48, ch = tid % 48;
      const u32x4 v = *reinterpret_cast<const u32x4*>(sh + part * 384 + ch * 8);
      st16_dev_u(rs, WS_QABS + ((part * H + h) * DC + 384 * half + ch * 8) * 2, v);
    }
    publish(p.ws, C_B1);
  }
  STAMP(1);
  {
    u32x4 raw[24];
    const u16* cp = p.Bc + (size_t)(128 * bb + 16 * wave + r) * DC + 8 * c;
#pragma unroll
    for (int hf = 0; hf < 2; ++hf)
#pragma unroll
      for (int rg = 0; rg < 2; ++rg)
#pragma unroll
        for (int i = 0; i < 6; ++i) raw[hf * 12 + rg * 6 + i] = ld16(cp + (size_t)(8 * rg) * DC + 384 * hf + 64 * i);
#pragma unroll
    for (int hf = 0; hf < 2; ++hf) {
#pragma unroll
      for (int rg = 0; rg < 2; ++rg)
#pragma unroll
        for (int i = 0; i < 6; ++i)
          *reinterpret_cast<u32x4*>(lds + L_BIG + (16 * wave + 8 * rg + r) * CROW + (64 * i + 8 * c) * 2) = raw[hf * 12 + rg * 6 + i];
      asm volatile("s_waitcnt lgkmcnt(0)" ::: "memory");
#pragma unroll
      for (int s = 0; s < 12; ++s) cf[12 * hf + s] = *reinterpret_cast<const u32x4*>(lds + L_BIG + (16 * wave + r16) * CROW + (32 * s + 8 * g) * 2);
      asm volatile("s_waitcnt lgkmcnt(0)" ::: "memory");
    }
  }
  wait_counters(p.ws, C_B1, 1, NB);
  STAMP(2);
#pragma unroll
  for (int i = 0; i < 12; ++i) {
    const int q = tid + 512 * i;
    const int row = q / 96, ch = q % 96;
    const u32x4 v = ld16_dev_u(rs, WS_QABS + q * 16);
    *reinterpret_cast<u32x4*>(lds + L_BIG + row * QROW + ch * 16) = v;
  }
  u32x4 wv[6][3];
#pragma unroll
  for (int t = 0; t < 6; ++t) {
    const u16* q_ = p.Bwuv + (size_t)(384 * half + 48 * wave + 8 * t + r) * HD + h * DH + c * 8;
#pragma unroll
    for (int i = 0; i < 3; ++i) wv[t][i] = ld16(q_ + i * 64);
  }
  __syncthreads();
  f32x4 sc[2] = {f32x4{0.f, 0.f, 0.f, 0.f}, f32x4{0.f, 0.f, 0.f, 0.f}};
#pragma unroll
  for (int s = 0; s < 24; ++s) {
#pragma unroll
    for (int ht = 0; ht < 2; ++ht) {
      const s16x8 bh = *reinterpret_cast<const s16x8*>(lds + L_BIG + (16 * ht + r16) * QROW + (32 * s + 8 * g) * 2);
      const s16x8 bl = *reinterpret_cast<const s16x8*>(lds + L_BIG + (32 + 16 * ht + r16) * QROW + (32 * s + 8 * g) * 2);
      sc[ht] = __builtin_amdgcn_mfma_f32_16x16x32_bf16(__builtin_bit_cast(s16x8, cf[s]), bh, sc[ht], 0, 0, 0);
      sc[ht] = __builtin_amdgcn_mfma_f32_16x16x32_bf16(__builtin_bit_cast(s16x8, cf[s]), bl, sc[ht], 0, 0, 0);
    }
  }
  float* wmax = reinterpret_cast<float*>(lds + L_ST);
  float* wsum = wmax + 256;
#pragma unroll
  for (int ht = 0; ht < 2; ++ht) {
#pragma unroll
    for (int k = 0; k < 4; ++k) sc[ht][k] = bf16r(bf16r(sc[ht][k]) * SCALE);
    float mx = fmaxf(fmaxf(sc[ht][0], sc[ht][1]), fmaxf(sc[ht][2], sc[ht][3]));
    mx = fmaxf(mx, __shfl_xor(mx, 16)); mx = fmaxf(mx, __shfl_xor(mx, 32));
    if (g == 0) wmax[wave * 32 + 16 * ht + r16] = mx;
  }
  __syncthreads();
#pragma unroll
  for (int ht = 0; ht < 2; ++ht) {
    float M = -INFINITY;
#pragma unroll
    for (int w8 = 0; w8 < 8; ++w8) M = fmaxf(M, wmax[w8 * 32 + 16 * ht + r16]);
    float e[4], sum = 0.f;
#pragma unroll
    for (int k = 0; k < 4; ++k) { e[k] = __builtin_amdgcn_exp2f((sc[ht][k] - M) * LOG2E); sum += e[k]; }
    sum += __shfl_xor(sum, 16); sum += __shfl_xor(sum, 32);
    if (g == 0) wsum[wave * 32 + 16 * ht + r16] = sum;
    u32x2 pk = {(unsigned)f2bf(e[0]) | ((unsigned)f2bf(e[1]) << 16), (unsigned)f2bf(e[2]) | ((unsigned)f2bf(e[3]) << 16)};
    *reinterpret_cast<u32x2*>(lds + L_P + (16 * ht + r16) * PROW + (16 * wave + 4 * g) * 2) = pk;
  }
#pragma unroll
  for (int s = 0; s < 12; ++s) *reinterpret_cast<u32x4*>(lds + L_BIG + (16 * wave + r16) * CROW + (32 * s + 8 * g) * 2) = cf[s];
  __syncthreads();
  if (tid < 32) {
    float M = -INFINITY, L = 0.f;
#pragma unroll
    for (int w8 = 0; w8 < 8; ++w8) { M = fmaxf(M, wmax[w8 * 32 + tid]); L += wsum[w8 * 32 + tid]; }
    st8_dev(p.ws, WS_BSTAT + (bb * H + tid) * 8, M, L);
  }
  {
    s16x8 pf[2][4];
#pragma unroll
    for (int ht = 0; ht < 2; ++ht)
#pragma unroll
      for (int ks = 0; ks < 4; ++ks) pf[ht][ks] = *reinterpret_cast<const s16x8*>(lds + L_P + (16 * ht + r16) * PROW + (32 * ks + 8 * g) * 2);
    const int q4 = r16 >> 2, p4 = r16 & 3;
#pragma unroll
    for (int ch = 0; ch < 2; ++ch) {
      if (ch == 1) {
        __syncthreads();
#pragma unroll
        for (int s = 0; s < 12; ++s) *reinterpret_cast<u32x4*>(lds + L_BIG + (16 * wave + r16) * CROW + (32 * s + 8 * g) * 2) = cf[12 + s];
        __syncthreads();
      }
#pragma unroll
      for (int ct = 0; ct < 3; ++ct) {
        f32x4 d[2] = {f32x4{0.f, 0.f, 0.f, 0.f}, f32x4{0.f, 0.f, 0.f, 0.f}};
#pragma unroll
        for (int ks = 0; ks < 4; ++ks) {
          const unsigned char* a0 = lds + L_BIG + (32 * ks + 8 * g + q4) * CROW + (48 * wave + 16 * ct + 4 * p4) * 2;
          typedef short v4i16_t __attribute__((ext_vector_type(4)));
          const v4i16_t t0 = __builtin_amdgcn_ds_read_tr16_b64_v4i16((LAS v4i16_t*)(a0));
          const v4i16_t t1 = __builtin_amdgcn_ds_read_tr16_b64_v4i16((LAS v4i16_t*)(a0 + 4 * CROW));
          const s16x8 af = {t0[0], t0[1], t0[2], t0[3], t1[0], t1[1], t1[2], t1[3]};
#pragma unroll
          for (int ht = 0; ht < 2; ++ht) d[ht] = __builtin_amdgcn_mfma_f32_16x16x32_bf16(af, pf[ht][ks], d[ht], 0, 0, 0);
        }
#pragma unroll
        for (int ht = 0; ht < 2; ++ht)
          st16_dev(rs, WS_BCTX + (unsigned)(((bb * H + 16 * ht + r16) * DC + 384 * ch + 48 * wave + 16 * ct + 4 * g) * 4), d[ht]);
      }
    }
  }
  publish(p.ws, C_B2);
  STAMP(3);
  wait_counters(p.ws, C_B2, 1, NB);
  STAMP(4);
  {
    float* coef = reinterpret_cast<float*>(lds + L_ST);
    float* part = reinterpret_cast<float*>(lds + L_BIG);
    float* red = reinterpret_cast<float*>(lds + L_RED);
    const int jg = tid / 96, q = tid % 96;
    f32x4 v[16];
    if (tid < 384) {
#pragma unroll
      for (int j = 0; j < 16; ++j) v[j] = ld16_dev(rs, WS_BCTX + (unsigned)((((16 * jg + j) * H + h) * DC + 384 * half + 4 * q) * 4));
    }
    if (tid >= 448) {
      const int j = tid - 448;
      const f32x2 ml = ld8_dev(p.ws, WS_BSTAT + (j * H + h) * 8);
      float M = ml[0];
#pragma unroll
      for (int o = 1; o < 64; o <<= 1) M = fmaxf(M, __shfl_xor(M, o));
      const float wt = __builtin_amdgcn_exp2f((ml[0] - M) * LOG2E);
      float L = wt * ml[1];
#pragma unroll
      for (int o = 1; o < 64; o <<= 1) L += __shfl_xor(L, o);
      coef[j] = wt / L;
    }
    __syncthreads();
    if (tid < 384) {
      f32x4 a = {0.f, 0.f, 0.f, 0.f};
#pragma unroll
      for (int j = 0; j < 16; ++j) a += coef[16 * jg + j] * v[j];
      *reinterpret_cast<f32x4*>(part + jg * 384 + 4 * q) = a;
    }
    __syncthreads();
    float acc[24];
#pragma unroll
    for (int k = 0; k < 24; ++k) acc[k] = 0.f;
#pragma unroll
    for (int t = 0; t < 6; ++t) { const int row = 48 * wave + 8 * t + r; fma_row(acc, (part[row] + part[384 + row]) + (part[768 + row] + part[1152 + row]), wv[t]); }
#pragma unroll
    for (int k = 0; k < 24; ++k) { float x = acc[k]; x += dpp<ROR8>(x); x += __shfl_xor(x, 16); x += __shfl_xor(x, 32); acc[k] = x; }
    if (r == 0) {
#pragma unroll
      for (int i = 0; i < 3; ++i) {
        *reinterpret_cast<f32x4*>(red + wave * 192 + i * 64 + c * 8) = f32x4{acc[i * 8], acc[i * 8 + 1], acc[i * 8 + 2], acc[i * 8 + 3]};
        *reinterpret_cast<f32x4*>(red + wave * 192 + i * 64 + c * 8 + 4) = f32x4{acc[i * 8 + 4], acc[i * 8 + 5], acc[i * 8 + 6], acc[i * 8 + 7]};
      }
    }
    __syncthreads();
    if (tid < 192) {
      float s = 0.f;
#pragma unroll
      for (int w8 = 0; w8 < 8; ++w8) s += red[w8 * 192 + tid];
      const unsigned long long gv = (1ull << 32) | (unsigned long long)__builtin_bit_cast(unsigned, s);
      __hip_atomic_store((GAS unsigned long long*)(p.ws + WS_XG + ((h * 2 + half) * DH + tid) * 8), gv, __ATOMIC_RELAXED, __HIP_MEMORY_SCOPE_AGENT);
    }
  }
  STAMP(5);
  if (bb < 40) {
    float* buf = reinterpret_cast<float*>(lds + L_BIG);
    float* red = reinterpret_cast<float*>(lds + L_RED);
    wait_counters(p.ws, C_AOUT, 8, NA / 8);
    STAMP(7);
    {
      const int j = tid >> 3, q = tid & 7;
      f32x4 v[3];
#pragma unroll
      for (int i = 0; i < 3; ++i) v[i] = ld16_dev(rs, WS_AOUT + ((j + 64 * i) * 2 * DM + 32 * bb + 4 * q) * 4);
#pragma unroll
      for (int i = 0; i < 3; ++i) *reinterpret_cast<f32x4*>(buf + (j + 64 * i) * 32 + q * 4) = v[i];
    }
    __syncthreads();
    if (tid < 256) {
      const int part = tid >> 5, col = tid & 31; float s = 0.f;
#pragma unroll
      for (int j = 0; j < 24; ++j) s += buf[(part * 24 + j) * 32 + col];
      red[tid] = s;
    }
    __syncthreads();
    if (tid < 32) {
      float s = 0.f;
#pragma unroll
      for (int j = 0; j < 8; ++j) s += red[j * 32 + tid];
      p.out[32 * bb + tid] = f2bf(s);
    }
  }
  STAMP(8);
}

__global__ void __launch_bounds__(NTHR, 2) fused_decode_kernel(Params p) {
  __shared__ __attribute__((aligned(16))) unsigned char lds[LDS_BYTES];
  const int b = blockIdx.x;
  STAMP(0);
  if (b < NA) {
    const int x = b & 7, idx = b >> 3;
    const int h = x * 4 + idx / ASPLIT, split = idx % ASPLIT;
    a_role(p, lds, h * ASPLIT + split, h, split);
  } else {
    const int bb = b - NA;
    b_role(p, lds, bb, (bb & 7) * 4 + (bb >> 4), (bb >> 3) & 1);
  }
}

extern "C" void kernel_launch(void* const* d_in, const int* in_sizes, int n_in,
                              void* d_out, int out_size, void* d_ws, size_t ws_size,
                              hipStream_t stream) {
  Params p;
  p.Ax = (const u16*)d_in[0]; p.Ak = (const u16*)d_in[1]; p.Av = (const u16*)d_in[2]; p.Awq = (const u16*)d_in[3]; p.Awo = (const u16*)d_in[4];
  p.Bx = (const u16*)d_in[5]; p.Bc = (const u16*)d_in[6]; p.Bwq = (const u16*)d_in[7]; p.Bwuk = (const u16*)d_in[8]; p.Bwuv = (const u16*)d_in[9]; p.Bwo = (const u16*)d_in[10];
  p.out = (u16*)d_out; p.ws = (unsigned char*)d_ws;
  (void)hipMemsetAsync((char*)d_ws + WS_CNT, 0, CNT_BYTES, stream);
  fused_decode_kernel<<<NA + NB, NTHR, 0, stream>>>(p);
}
```

```cpp
#include <hip/hip_runtime.h>
#include <hip/hip_bf16.h>
#include <stdint.h>

#define GAS __attribute__((address_space(1)))
#define LAS __attribute__((address_space(3)))
typedef unsigned u32x4 __attribute__((ext_vector_type(4)));
typedef unsigned u32x2 __attribute__((ext_vector_type(2)));
typedef float f32x4 __attribute__((ext_vector_type(4)));
typedef float f32x2 __attribute__((ext_vector_type(2)));
typedef short s16x8 __attribute__((ext_vector_type(8)));
typedef short s16x4 __attribute__((ext_vector_type(4)));
typedef __bf16 bf16x2_t __attribute__((ext_vector_type(2)));
typedef unsigned short u16;

constexpr int H = 32, SK = 8192, DH = 192, DM = 640, DC = 768, HD = H * DH;
constexpr int NA = 192, NB = 64, NTHR = 512, ASPLIT = 6;
constexpr float SCALE = 0.07216878364870322f;
constexpr float LOG2E = 1.4426950408889634f;

constexpr unsigned CNT_STRIDE = 128;
constexpr unsigned WS_CNT = 0;
constexpr unsigned WS_QG = 48 * CNT_STRIDE;
constexpr unsigned CNT_BYTES = WS_QG + 2 * 3072 * 8;
constexpr int C_ZB = 8;
constexpr int C_B1 = 9, C_B2 = 10;
constexpr unsigned WS_XG = 65536;
constexpr unsigned WS_PG = WS_XG + H * 2 * DH * 8;
constexpr unsigned WS_OG = WS_PG + NA * 200 * 8;
constexpr unsigned WS_QABS = WS_OG + 40 * NA * 32 * 8;
constexpr unsigned WS_BSTAT = WS_QABS + 2 * H * DC * 2;
constexpr unsigned WS_BCTX = WS_BSTAT + NB * H * 2 * 4;
constexpr unsigned WS_END = WS_BCTX + NB * H * DC * 4;
static_assert(CNT_BYTES <= WS_XG && CNT_BYTES % 16 == 0, "ws");
constexpr int ZPER = (int)((WS_QABS - WS_XG) / 16 / NB), ZPT = (ZPER + 511) / 512;
static_assert((WS_QABS - WS_XG) % (16 * NB) == 0, "ws");

constexpr int QROW = 1552;
constexpr int CROW = 784;
constexpr int PROW = 272;
constexpr int L_BIG = 0;
constexpr int L_P = 100352;
constexpr int L_ST = L_P + 32 * PROW;
constexpr int L_RED = L_ST + 2048;
constexpr int L_Q = L_RED + 16384;
constexpr int LDS_BYTES = L_Q + 1024;
constexpr int LA_WO = 0;
constexpr int LA_WOB = 40960;
constexpr int LA_RED = 81920;
constexpr int LA_Q = LA_RED + 24576;
static_assert(LA_Q + 4096 <= LDS_BYTES, "lds");

#define STAMP(i) do { } while (0)
struct Params {
  const u16* Ax; const u16* Ak; const u16* Av; const u16* Awq; const u16* Awo;
  const u16* Bx; const u16* Bc; const u16* Bwq; const u16* Bwuk; const u16* Bwuv; const u16* Bwo;
  u16* out; unsigned char* ws;
};

__device__ __forceinline__ unsigned f2u(float x) { return __builtin_bit_cast(unsigned, x); }
__device__ __forceinline__ float u2f(unsigned x) { return __builtin_bit_cast(float, x); }
__device__ __forceinline__ float bf_lo(unsigned u) { return __builtin_bit_cast(float, u << 16); }
__device__ __forceinline__ float bf_hi(unsigned u) { return __builtin_bit_cast(float, u & 0xffff0000u); }
__device__ __forceinline__ float bf2f(u16 u) { return __builtin_bit_cast(float, (unsigned)u << 16); }
__device__ __forceinline__ u16 f2bf(float x) { __bf16 b = (__bf16)x; return __builtin_bit_cast(u16, b); }
__device__ __forceinline__ float bf16r(float x) { return bf2f(f2bf(x)); }
__device__ __forceinline__ float dot2(unsigned a, unsigned b, float acc) {
  return __builtin_amdgcn_fdot2_f32_bf16(__builtin_bit_cast(bf16x2_t, a), __builtin_bit_cast(bf16x2_t, b), acc, false);
}
template <int CTRL> __device__ __forceinline__ float dpp(float x) {
  return __builtin_bit_cast(float, __builtin_amdgcn_mov_dpp(__builtin_bit_cast(int, x), CTRL, 0xf, 0xf, true));
}
constexpr int XOR1 = 0xB1, XOR2 = 0x4E, HMIRROR = 0x141, ROR8 = 0x128, ROR4 = 0x124;
__device__ __forceinline__ float sum8(float s) {
  s += dpp<XOR1>(s); s += dpp<XOR2>(s); s += dpp<HMIRROR>(s); return s;
}
__device__ __forceinline__ u32x4 ld16(const u16* p) { return __builtin_nontemporal_load(reinterpret_cast<const u32x4*>(p)); }

__device__ __forceinline__ __amdgpu_buffer_rsrc_t ws_rsrc(unsigned char* ws) {
  return __builtin_amdgcn_make_buffer_rsrc(ws, 0, (int)WS_END, 0x00020000);
}
__device__ __forceinline__ void st16_dev(__amdgpu_buffer_rsrc_t r, unsigned off, f32x4 v) {
  __builtin_amdgcn_raw_buffer_store_b128(__builtin_bit_cast(u32x4, v), r, off, 0, 16);
}
__device__ __forceinline__ void st16_dev_u(__amdgpu_buffer_rsrc_t r, unsigned off, u32x4 v) {
  __builtin_amdgcn_raw_buffer_store_b128(v, r, off, 0, 16);
}
__device__ __forceinline__ f32x4 ld16_dev(__amdgpu_buffer_rsrc_t r, unsigned off) {
  return __builtin_bit_cast(f32x4, __builtin_amdgcn_raw_buffer_load_b128(r, off, 0, 16));
}
__device__ __forceinline__ u32x4 ld16_dev_u(__amdgpu_buffer_rsrc_t r, unsigned off) {
  return __builtin_amdgcn_raw_buffer_load_b128(r, off, 0, 16);
}
__device__ __forceinline__ void st4_dev(unsigned char* ws, unsigned off, float v) {
  __hip_atomic_store((GAS float*)(ws + off), v, __ATOMIC_RELAXED, __HIP_MEMORY_SCOPE_AGENT);
}
__device__ __forceinline__ void st4_dev_u(unsigned char* ws, unsigned off, unsigned v) {
  __hip_atomic_store((GAS unsigned*)(ws + off), v, __ATOMIC_RELAXED, __HIP_MEMORY_SCOPE_AGENT);
}
__device__ __forceinline__ float ld4_dev(unsigned char* ws, unsigned off) {
  return __hip_atomic_load((GAS float*)(ws + off), __ATOMIC_RELAXED, __HIP_MEMORY_SCOPE_AGENT);
}
__device__ __forceinline__ void st8_dev(unsigned char* ws, unsigned off, float a, float b) {
  f32x2 v = {a, b};
  __hip_atomic_store((GAS unsigned long long*)(ws + off), __builtin_bit_cast(unsigned long long, v), __ATOMIC_RELAXED, __HIP_MEMORY_SCOPE_AGENT);
}
__device__ __forceinline__ f32x2 ld8_dev(unsigned char* ws, unsigned off) {
  unsigned long long v = __hip_atomic_load((GAS unsigned long long*)(ws + off), __ATOMIC_RELAXED, __HIP_MEMORY_SCOPE_AGENT);
  return __builtin_bit_cast(f32x2, v);
}
__device__ __forceinline__ void publish(unsigned char* ws, int cnt) {
  asm volatile("s_waitcnt vmcnt(0)" ::: "memory");
  __syncthreads();
  if (threadIdx.x == 0)
    __hip_atomic_fetch_add((GAS unsigned*)(ws + WS_CNT + cnt * CNT_STRIDE), 1u, __ATOMIC_RELAXED, __HIP_MEMORY_SCOPE_AGENT);
}
__device__ __forceinline__ void wait_counters(unsigned char* ws, int cnt, int n, unsigned want) {
  if (threadIdx.x < 64) {
    const int lane = threadIdx.x;
    GAS unsigned* p = (GAS unsigned*)(ws + WS_CNT + (cnt + (lane < n ? lane : 0)) * CNT_STRIDE);
    for (unsigned spins = 0; spins < (1u << 22); ++spins) {
      const unsigned v = __hip_atomic_load(p, __ATOMIC_RELAXED, __HIP_MEMORY_SCOPE_AGENT);
      if (__all(v >= want)) break;
      __builtin_amdgcn_s_sleep(2);
    }
  }
  __syncthreads();
}

__device__ __forceinline__ unsigned long long granule(float v) { return (1ull << 32) | (unsigned long long)__builtin_bit_cast(unsigned, v); }
__device__ __forceinline__ void st_granule(unsigned char* ws, unsigned off, unsigned long long gv) {
  __hip_atomic_store((GAS unsigned long long*)(ws + off), gv, __ATOMIC_RELAXED, __HIP_MEMORY_SCOPE_AGENT);
}
__device__ __forceinline__ unsigned long long ld_granule(unsigned char* ws, unsigned off) {
  return __hip_atomic_load((const GAS unsigned long long*)(ws + off), __ATOMIC_RELAXED, __HIP_MEMORY_SCOPE_AGENT);
}
__device__ __forceinline__ void gather_q(unsigned char* ws, int which, int h, unsigned* lq, unsigned (&qpk)[12]) {
  const int tid = threadIdx.x, c = tid & 7;
  if (tid < 64) {
    const unsigned goff = WS_QG + (which * 3072 + h * 96 + 2 * (tid < 48 ? tid : 0)) * 8;
    unsigned long long a = 0, b = 0;
    for (unsigned spins = 0; spins < (1u << 22); ++spins) {
      a = ld_granule(ws, goff);
      b = ld_granule(ws, goff + 8);
      if (__all((a >> 32) == 1ull && (b >> 32) == 1ull)) break;
      __builtin_amdgcn_s_sleep(2);
    }
    if (tid < 48) { lq[2 * tid] = (unsigned)a; lq[2 * tid + 1] = (unsigned)b; }
  }
  __syncthreads();
#pragma unroll
  for (int i = 0; i < 3; ++i)
#pragma unroll
    for (int k = 0; k < 4; ++k) qpk[i * 4 + k] = lq[i * 32 + c * 4 + k];
}
__device__ __forceinline__ unsigned zero_cnt_load(unsigned char* ws) {
  return __hip_atomic_load((const GAS unsigned*)(ws + WS_CNT + C_ZB * CNT_STRIDE), __ATOMIC_RELAXED, __HIP_MEMORY_SCOPE_AGENT);
}
__device__ __forceinline__ void zero_cnt_wait(unsigned char* ws, unsigned z) {
  for (unsigned spins = 0; spins < (1u << 22); ++spins) {
    if (__all(z >= (unsigned)NB)) break;
    __builtin_amdgcn_s_sleep(2);
    z = zero_cnt_load(ws);
  }
}

__device__ __forceinline__ void fma_row(float (&acc)[24], float s, const u32x4 (&w)[3]) {
#pragma unroll
  for (int i = 0; i < 3; ++i)
#pragma unroll
    for (int k = 0; k < 4; ++k) {
      acc[i * 8 + 2 * k] = fmaf(s, bf_lo(w[i][k]), acc[i * 8 + 2 * k]);
      acc[i * 8 + 2 * k + 1] = fmaf(s, bf_hi(w[i][k]), acc[i * 8 + 2 * k + 1]);
    }
}
__device__ __forceinline__ float dot_row(const u32x4 (&w)[3], const unsigned (&q)[12]) {
  float s0 = 0.f, s1 = 0.f, s2 = 0.f;
#pragma unroll
  for (int k = 0; k < 4; ++k) { s0 = dot2(w[0][k], q[k], s0); s1 = dot2(w[1][k], q[4 + k], s1); s2 = dot2(w[2][k], q[8 + k], s2); }
  return (s0 + s1) + s2;
}
__device__ __forceinline__ void a_role(const Params& p, unsigned char* lds, int wg, int h, int split) {
  const int tid = threadIdx.x, wave = __builtin_amdgcn_readfirstlane(tid >> 6), lane = tid & 63, r = lane >> 3, c = lane & 7;
  const int gw = split * 8 + wave;
  const int t0 = (1024 * gw) / 48, t1 = (1024 * (gw + 1)) / 48, nt = t1 - t0;
  const u16* kb = p.Ak + ((size_t)h * SK + r) * DH + c * 8;
  const u16* vb = p.Av + ((size_t)h * SK + r) * DH + c * 8;
  const __amdgpu_buffer_rsrc_t rs = ws_rsrc(p.ws);
  u32x4 kr[4][3], vr[4][3];
  unsigned qpk[12];
  float* red = reinterpret_cast<float*>(lds + LA_RED);
#define A_LOAD(slot, idx) do { const int tt_ = t0 + ((idx) < nt ? (idx) : nt - 1); const size_t o_ = (size_t)tt_ * 8 * DH; \
    _Pragma("unroll") for (int i_ = 0; i_ < 3; ++i_) kr[slot][i_] = ld16(kb + o_ + i_ * 64); \
    _Pragma("unroll") for (int i_ = 0; i_ < 3; ++i_) vr[slot][i_] = ld16(vb + o_ + i_ * 64); } while (0)
  {
    const int which = split >= 3 ? 1 : 0, sl = split - 3 * which;
    const u16* x = which ? p.Bx : p.Ax;
    const u16* W = (which ? p.Bwq : p.Awq) + h * DH + 64 * sl + c * 8;
    float xa[10]; u32x4 wa[10];
#pragma unroll
    for (int t = 0; t < 10; ++t) xa[t] = bf2f(x[wave * 80 + t * 8 + r]);
#pragma unroll
    for (int t = 0; t < 10; ++t) wa[t] = ld16(W + (size_t)(wave * 80 + t * 8 + r) * HD);
#pragma unroll
    for (int d = 0; d < 4; ++d) A_LOAD(d, d);
    float a[8];
#pragma unroll
    for (int k = 0; k < 8; ++k) a[k] = 0.f;
#pragma unroll
    for (int t = 0; t < 10; ++t)
#pragma unroll
      for (int k = 0; k < 4; ++k) { a[2 * k] = fmaf(xa[t], bf_lo(wa[t][k]), a[2 * k]); a[2 * k + 1] = fmaf(xa[t], bf_hi(wa[t][k]), a[2 * k + 1]); }
#pragma unroll
    for (int k = 0; k < 8; ++k) { float v = a[k]; v += dpp<ROR8>(v); v += __shfl_xor(v, 16); v += __shfl_xor(v, 32); a[k] = v; }
    if (r == 0) {
      *reinterpret_cast<f32x4*>(red + wave * 64 + c * 8) = f32x4{a[0], a[1], a[2], a[3]};
      *reinterpret_cast<f32x4*>(red + wave * 64 + c * 8 + 4) = f32x4{a[4], a[5], a[6], a[7]};
    }
    __syncthreads();
    if (tid < 32) {
      float s0 = 0.f, s1 = 0.f;
#pragma unroll
      for (int w8 = 0; w8 < 8; ++w8) { s0 += red[w8 * 64 + 2 * tid]; s1 += red[w8 * 64 + 2 * tid + 1]; }
      const unsigned long long gv = (1ull << 32) | (unsigned long long)((unsigned)f2bf(s0) | ((unsigned)f2bf(s1) << 16));
      __hip_atomic_store((GAS unsigned long long*)(p.ws + WS_QG + (which * 3072 + h * 96 + 32 * sl + tid) * 8), gv, __ATOMIC_RELAXED, __HIP_MEMORY_SCOPE_AGENT);
    }
  }
  {
    u32x4 t[10];
    const u16* wo = p.Awo + (size_t)(h * DH + 32 * split) * DM;
    const u16* wob = p.Bwo + (size_t)(h * DH + 32 * split) * DM;
#pragma unroll
    for (int i = 0; i < 5; ++i) { t[i] = ld16(wo + (tid + 512 * i) * 8); t[5 + i] = ld16(wob + (tid + 512 * i) * 8); }
#pragma unroll
    for (int i = 0; i < 5; ++i) {
      *reinterpret_cast<u32x4*>(lds + LA_WO + (tid + 512 * i) * 16) = t[i];
      *reinterpret_cast<u32x4*>(lds + LA_WOB + (tid + 512 * i) * 16) = t[5 + i];
    }
  }
  gather_q(p.ws, 0, h, reinterpret_cast<unsigned*>(lds + LA_Q), qpk);
  STAMP(1);
  float m = -INFINITY, l = 0.f, acc[24];
#pragma unroll
  for (int k = 0; k < 24; ++k) acc[k] = 0.f;
#define A_STEP(slot, idx) do { \
    float s_ = sum8(dot_row(kr[slot], qpk)); \
    s_ = bf16r(bf16r(s_) * SCALE); \
    s_ = ((idx) < nt) ? s_ : -INFINITY; \
    const float mn_ = fmaxf(m, s_); \
    const float al_ = __builtin_amdgcn_exp2f((m - mn_) * LOG2E), pp_ = __builtin_amdgcn_exp2f((s_ - mn_) * LOG2E); \
    l = fmaf(l, al_, pp_); m = mn_; \
    _Pragma("unroll") for (int k_ = 0; k_ < 24; ++k_) acc[k_] *= al_; \
    fma_row(acc, pp_, vr[slot]); } while (0)
  for (int it = 0; it < 5; ++it) {
#pragma unroll
    for (int d = 0; d < 4; ++d) { A_STEP(d, it * 4 + d); A_LOAD(d, it * 4 + d + 4); }
  }
  const unsigned zc = zero_cnt_load(p.ws);
#pragma unroll
  for (int d = 0; d < 4; ++d) A_STEP(d, 20 + d);
  zero_cnt_wait(p.ws, zc);
#undef A_STEP
#undef A_LOAD
  STAMP(2);
  {
    float M = m; M = fmaxf(M, dpp<ROR8>(M)); M = fmaxf(M, __shfl_xor(M, 16)); M = fmaxf(M, __shfl_xor(M, 32));
    const float f = __builtin_amdgcn_exp2f((m - M) * LOG2E);
    l *= f; l += dpp<ROR8>(l); l += __shfl_xor(l, 16); l += __shfl_xor(l, 32);
#pragma unroll
    for (int k = 0; k < 24; ++k) { float v = acc[k] * f; v += dpp<ROR8>(v); v += __shfl_xor(v, 16); v += __shfl_xor(v, 32); acc[k] = v; }
    if (r == 0) {
#pragma unroll
      for (int i = 0; i < 3; ++i) {
        *reinterpret_cast<f32x4*>(red + wave * 200 + i * 64 + c * 8) = f32x4{acc[i * 8], acc[i * 8 + 1], acc[i * 8 + 2], acc[i * 8 + 3]};
        *reinterpret_cast<f32x4*>(red + wave * 200 + i * 64 + c * 8 + 4) = f32x4{acc[i * 8 + 4], acc[i * 8 + 5], acc[i * 8 + 6], acc[i * 8 + 7]};
      }
      if (c == 0) { red[wave * 200 + 192] = M; red[wave * 200 + 193] = l; }
    }
    __syncthreads();
    if (tid < 194) {
      float MM = -INFINITY;
#pragma unroll
      for (int w8 = 0; w8 < 8; ++w8) MM = fmaxf(MM, red[w8 * 200 + 192]);
      float o = 0.f;
      const int src = tid < 192 ? tid : 193;
#pragma unroll
      for (int w8 = 0; w8 < 8; ++w8) o = fmaf(__builtin_amdgcn_exp2f((red[w8 * 200 + 192] - MM) * LOG2E), red[w8 * 200 + src], o);
      st_granule(p.ws, WS_PG + (wg * 200 + tid) * 8, granule(tid == 192 ? MM : o));
    }
  }
  STAMP(3);
  float* gat = reinterpret_cast<float*>(lds + LA_Q);
  float* sv = gat + 512;
  if (tid < 320) {
    unsigned off;
    if (tid < 256) { const int t_ = tid < 204 ? tid : 0, j = t_ / 34, k = t_ % 34; off = WS_PG + ((h * ASPLIT + j) * 200 + (k < 32 ? 32 * split + k : 160 + k)) * 8; }
    else off = WS_XG + ((h * 2 + ((tid >> 5) & 1)) * DH + 32 * split + (tid & 31)) * 8;
    unsigned long long a = 0;
    for (unsigned spins = 0; spins < (1u << 22); ++spins) {
      a = ld_granule(p.ws, off);
      if (__all((a >> 32) == 1ull)) break;
      __builtin_amdgcn_s_sleep(1);
    }
    gat[tid] = __builtin_bit_cast(float, (unsigned)a);
  }
  __syncthreads();
  STAMP(5);
  if (tid < 32) {
    float MM = -INFINITY;
#pragma unroll
    for (int j = 0; j < ASPLIT; ++j) MM = fmaxf(MM, gat[j * 34 + 32]);
    float o = 0.f, L = 0.f;
#pragma unroll
    for (int j = 0; j < ASPLIT; ++j) { const float wt = __builtin_amdgcn_exp2f((gat[j * 34 + 32] - MM) * LOG2E); o = fmaf(wt, gat[j * 34 + tid], o); L = fmaf(wt, gat[j * 34 + 33], L); }
    sv[tid] = bf16r(o / L);
  } else if (tid < 64) {
    sv[tid] = bf16r(gat[256 + tid - 32] + gat[256 + tid]);
  }
  __syncthreads();
  {
    float* red2 = red;
    const int chunk = tid % 80, rg = tid / 80;
    if (tid < 320) {
      float a[8], b[8];
#pragma unroll
      for (int k = 0; k < 8; ++k) { a[k] = 0.f; b[k] = 0.f; }
#pragma unroll
      for (int i = 0; i < 8; ++i) {
        const float sa = sv[rg * 8 + i], sb = sv[32 + rg * 8 + i];
        const u32x4 wa = *reinterpret_cast<const u32x4*>(lds + LA_WO + (rg * 8 + i) * 1280 + chunk * 16);
        const u32x4 wb = *reinterpret_cast<const u32x4*>(lds + LA_WOB + (rg * 8 + i) * 1280 + chunk * 16);
#pragma unroll
        for (int k = 0; k < 4; ++k) {
          a[2 * k] = fmaf(sa, bf_lo(wa[k]), a[2 * k]); a[2 * k + 1] = fmaf(sa, bf_hi(wa[k]), a[2 * k + 1]);
          b[2 * k] = fmaf(sb, bf_lo(wb[k]), b[2 * k]); b[2 * k + 1] = fmaf(sb, bf_hi(wb[k]), b[2 * k + 1]);
        }
      }
      *reinterpret_cast<f32x4*>(red2 + rg * 640 + chunk * 8) = f32x4{a[0], a[1], a[2], a[3]};
      *reinterpret_cast<f32x4*>(red2 + rg * 640 + chunk * 8 + 4) = f32x4{a[4], a[5], a[6], a[7]};
      *reinterpret_cast<f32x4*>(red2 + 2560 + rg * 640 + chunk * 8) = f32x4{b[0], b[1], b[2], b[3]};
      *reinterpret_cast<f32x4*>(red2 + 2560 + rg * 640 + chunk * 8 + 4) = f32x4{b[4], b[5], b[6], b[7]};
    }
    __syncthreads();
    if (tid < 320) {
      const float* src = red2 + (tid < 160 ? tid * 4 : 2560 + (tid - 160) * 4);
      f32x4 sum = *reinterpret_cast<const f32x4*>(src);
#pragma unroll
      for (int g = 1; g < 4; ++g) sum += *reinterpret_cast<const f32x4*>(src + g * 640);
      const unsigned off = WS_OG + (((tid >> 3) * NA + wg) * 32 + (tid & 7) * 4) * 8;
      st16_dev_u(rs, off, u32x4{f2u(sum[0]), 1u, f2u(sum[1]), 1u});
      st16_dev_u(rs, off + 16, u32x4{f2u(sum[2]), 1u, f2u(sum[3]), 1u});
    }
  }
  STAMP(6);
}

__device__ __forceinline__ void b_role(const Params& p, unsigned char* lds, int bb, int h, int half) {
  const int tid = threadIdx.x, wave = tid >> 6, lane = tid & 63, r = lane >> 3, c = lane & 7, r16 = lane & 15, g = lane >> 4;
  const __amdgpu_buffer_rsrc_t rs = ws_rsrc(p.ws);
  u32x4 cf[24];
  {
#pragma unroll
    for (int i = 0; i < ZPT; ++i) { const unsigned o = (unsigned)(bb * ZPER + tid + 512 * i); if (tid + 512 * i < ZPER) st16_dev_u(rs, WS_XG + o * 16, u32x4{0u, 0u, 0u, 0u}); }
    asm volatile("" ::: "memory");
    u32x4 w[6][3];
#pragma unroll
    for (int t = 0; t < 6; ++t) {
      const u16* q_ = p.Bwuk + (size_t)(384 * half + 48 * wave + 8 * t + r) * HD + h * DH + c * 8;
#pragma unroll
      for (int i = 0; i < 3; ++i) w[t][i] = ld16(q_ + i * 64);
    }
    asm volatile("s_waitcnt vmcnt(18)" ::: "memory");
    __syncthreads();
    if (tid == 64) __hip_atomic_fetch_add((GAS unsigned*)(p.ws + WS_CNT + C_ZB * CNT_STRIDE), 1u, __ATOMIC_RELAXED, __HIP_MEMORY_SCOPE_AGENT);
    unsigned qpk[12];
    gather_q(p.ws, 1, h, reinterpret_cast<unsigned*>(lds + L_Q), qpk);
    u16* sh = reinterpret_cast<u16*>(lds + L_RED);
#pragma unroll
    for (int t = 0; t < 6; ++t) {
      const float s = sum8(dot_row(w[t], qpk));
      const u16 hi = f2bf(s); const u16 lo = f2bf(s - bf2f(hi));
      if (c == 0) { sh[48 * wave + 8 * t + r] = hi; sh[384 + 48 * wave + 8 * t + r] = lo; }
    }
    __syncthreads();
    if (tid < 96) {
      const int part = tid / 48, ch = tid % 48;
      const u32x4 v = *reinterpret_cast<const u32x4*>(sh + part * 384 + ch * 8);
      st16_dev_u(rs, WS_QABS + ((part * H + h) * DC + 384 * half + ch * 8) * 2, v);
    }
    publish(p.ws, C_B1);
  }
  STAMP(1);
  {
    u32x4 raw[24];
    const u16* cp = p.Bc + (size_t)(128 * bb + 16 * wave + r) * DC + 8 * c;
#pragma unroll
    for (int hf = 0; hf < 2; ++hf)
#pragma unroll
      for (int rg = 0; rg < 2; ++rg)
#pragma unroll
        for (int i = 0; i < 6; ++i) raw[hf * 12 + rg * 6 + i] = ld16(cp + (size_t)(8 * rg) * DC + 384 * hf + 64 * i);
#pragma unroll
    for (int hf = 0; hf < 2; ++hf) {
#pragma unroll
      for (int rg = 0; rg < 2; ++rg)
#pragma unroll
        for (int i = 0; i < 6; ++i)
          *reinterpret_cast<u32x4*>(lds + L_BIG + (16 * wave + 8 * rg + r) * CROW + (64 * i + 8 * c) * 2) = raw[hf * 12 + rg * 6 + i];
      asm volatile("s_waitcnt lgkmcnt(0)" ::: "memory");
#pragma unroll
      for (int s = 0; s < 12; ++s) cf[12 * hf + s] = *reinterpret_cast<const u32x4*>(lds + L_BIG + (16 * wave + r16) * CROW + (32 * s + 8 * g) * 2);
      asm volatile("s_waitcnt lgkmcnt(0)" ::: "memory");
    }
  }
  wait_counters(p.ws, C_B1, 1, NB);
  STAMP(2);
#pragma unroll
  for (int i = 0; i < 12; ++i) {
    const int q = tid + 512 * i;
    const int row = q / 96, ch = q % 96;
    const u32x4 v = ld16_dev_u(rs, WS_QABS + q * 16);
    *reinterpret_cast<u32x4*>(lds + L_BIG + row * QROW + ch * 16) = v;
  }
  u32x4 wv[6][3];
#pragma unroll
  for (int t = 0; t < 6; ++t) {
    const u16* q_ = p.Bwuv + (size_t)(384 * half + 48 * wave + 8 * t + r) * HD + h * DH + c * 8;
#pragma unroll
    for (int i = 0; i < 3; ++i) wv[t][i] = ld16(q_ + i * 64);
  }
  __syncthreads();
  f32x4 sc[2] = {f32x4{0.f, 0.f, 0.f, 0.f}, f32x4{0.f, 0.f, 0.f, 0.f}};
#pragma unroll
  for (int s = 0; s < 24; ++s) {
#pragma unroll
    for (int ht = 0; ht < 2; ++ht) {
      const s16x8 bh = *reinterpret_cast<const s16x8*>(lds + L_BIG + (16 * ht + r16) * QROW + (32 * s + 8 * g) * 2);
      const s16x8 bl = *reinterpret_cast<const s16x8*>(lds + L_BIG + (32 + 16 * ht + r16) * QROW + (32 * s + 8 * g) * 2);
      sc[ht] = __builtin_amdgcn_mfma_f32_16x16x32_bf16(__builtin_bit_cast(s16x8, cf[s]), bh, sc[ht], 0, 0, 0);
      sc[ht] = __builtin_amdgcn_mfma_f32_16x16x32_bf16(__builtin_bit_cast(s16x8, cf[s]), bl, sc[ht], 0, 0, 0);
    }
  }
  float* wmax = reinterpret_cast<float*>(lds + L_ST);
  float* wsum = wmax + 256;
#pragma unroll
  for (int ht = 0; ht < 2; ++ht) {
#pragma unroll
    for (int k = 0; k < 4; ++k) sc[ht][k] = bf16r(bf16r(sc[ht][k]) * SCALE);
    float mx = fmaxf(fmaxf(sc[ht][0], sc[ht][1]), fmaxf(sc[ht][2], sc[ht][3]));
    mx = fmaxf(mx, __shfl_xor(mx, 16)); mx = fmaxf(mx, __shfl_xor(mx, 32));
    if (g == 0) wmax[wave * 32 + 16 * ht + r16] = mx;
  }
  __syncthreads();
#pragma unroll
  for (int ht = 0; ht < 2; ++ht) {
    float M = -INFINITY;
#pragma unroll
    for (int w8 = 0; w8 < 8; ++w8) M = fmaxf(M, wmax[w8 * 32 + 16 * ht + r16]);
    float e[4], sum = 0.f;
#pragma unroll
    for (int k = 0; k < 4; ++k) { e[k] = __builtin_amdgcn_exp2f((sc[ht][k] - M) * LOG2E); sum += e[k]; }
    sum += __shfl_xor(sum, 16); sum += __shfl_xor(sum, 32);
    if (g == 0) wsum[wave * 32 + 16 * ht + r16] = sum;
    u32x2 pk = {(unsigned)f2bf(e[0]) | ((unsigned)f2bf(e[1]) << 16), (unsigned)f2bf(e[2]) | ((unsigned)f2bf(e[3]) << 16)};
    *reinterpret_cast<u32x2*>(lds + L_P + (16 * ht + r16) * PROW + (16 * wave + 4 * g) * 2) = pk;
  }
#pragma unroll
  for (int s = 0; s < 12; ++s) *reinterpret_cast<u32x4*>(lds + L_BIG + (16 * wave + r16) * CROW + (32 * s + 8 * g) * 2) = cf[s];
  __syncthreads();
  if (tid < 32) {
    float M = -INFINITY, L = 0.f;
#pragma unroll
    for (int w8 = 0; w8 < 8; ++w8) { M = fmaxf(M, wmax[w8 * 32 + tid]); L += wsum[w8 * 32 + tid]; }
    st8_dev(p.ws, WS_BSTAT + (bb * H + tid) * 8, M, L);
  }
  {
    s16x8 pf[2][4];
#pragma unroll
    for (int ht = 0; ht < 2; ++ht)
#pragma unroll
      for (int ks = 0; ks < 4; ++ks) pf[ht][ks] = *reinterpret_cast<const s16x8*>(lds + L_P + (16 * ht + r16) * PROW + (32 * ks + 8 * g) * 2);
    const int q4 = r16 >> 2, p4 = r16 & 3;
#pragma unroll
    for (int ch = 0; ch < 2; ++ch) {
      if (ch == 1) {
        __syncthreads();
#pragma unroll
        for (int s = 0; s < 12; ++s) *reinterpret_cast<u32x4*>(lds + L_BIG + (16 * wave + r16) * CROW + (32 * s + 8 * g) * 2) = cf[12 + s];
        __syncthreads();
      }
#pragma unroll
      for (int ct = 0; ct < 3; ++ct) {
        f32x4 d[2] = {f32x4{0.f, 0.f, 0.f, 0.f}, f32x4{0.f, 0.f, 0.f, 0.f}};
#pragma unroll
        for (int ks = 0; ks < 4; ++ks) {
          const unsigned char* a0 = lds + L_BIG + (32 * ks + 8 * g + q4) * CROW + (48 * wave + 16 * ct + 4 * p4) * 2;
          typedef short v4i16_t __attribute__((ext_vector_type(4)));
          const v4i16_t t0 = __builtin_amdgcn_ds_read_tr16_b64_v4i16((LAS v4i16_t*)(a0));
          const v4i16_t t1 = __builtin_amdgcn_ds_read_tr16_b64_v4i16((LAS v4i16_t*)(a0 + 4 * CROW));
          const s16x8 af = {t0[0], t0[1], t0[2], t0[3], t1[0], t1[1], t1[2], t1[3]};
#pragma unroll
          for (int ht = 0; ht < 2; ++ht) d[ht] = __builtin_amdgcn_mfma_f32_16x16x32_bf16(af, pf[ht][ks], d[ht], 0, 0, 0);
        }
#pragma unroll
        for (int ht = 0; ht < 2; ++ht)
          st16_dev(rs, WS_BCTX + (unsigned)(((bb * H + 16 * ht + r16) * DC + 384 * ch + 48 * wave + 16 * ct + 4 * g) * 4), d[ht]);
      }
    }
  }
  publish(p.ws, C_B2);
  STAMP(3);
  zero_cnt_wait(p.ws, zero_cnt_load(p.ws));
  wait_counters(p.ws, C_B2, 1, NB);
  STAMP(4);
  {
    float* coef = reinterpret_cast<float*>(lds + L_ST);
    float* part = reinterpret_cast<float*>(lds + L_BIG);
    float* red = reinterpret_cast<float*>(lds + L_RED);
    const int jg = tid / 96, q = tid % 96;
    f32x4 v[16];
    if (tid < 384) {
#pragma unroll
      for (int j = 0; j < 16; ++j) v[j] = ld16_dev(rs, WS_BCTX + (unsigned)((((16 * jg + j) * H + h) * DC + 384 * half + 4 * q) * 4));
    }
    if (tid >= 448) {
      const int j = tid - 448;
      const f32x2 ml = ld8_dev(p.ws, WS_BSTAT + (j * H + h) * 8);
      float M = ml[0];
#pragma unroll
      for (int o = 1; o < 64; o <<= 1) M = fmaxf(M, __shfl_xor(M, o));
      const float wt = __builtin_amdgcn_exp2f((ml[0] - M) * LOG2E);
      float L = wt * ml[1];
#pragma unroll
      for (int o = 1; o < 64; o <<= 1) L += __shfl_xor(L, o);
      coef[j] = wt / L;
    }
    __syncthreads();
    if (tid < 384) {
      f32x4 a = {0.f, 0.f, 0.f, 0.f};
#pragma unroll
      for (int j = 0; j < 16; ++j) a += coef[16 * jg + j] * v[j];
      *reinterpret_cast<f32x4*>(part + jg * 384 + 4 * q) = a;
    }
    __syncthreads();
    float acc[24];
#pragma unroll
    for (int k = 0; k < 24; ++k) acc[k] = 0.f;
#pragma unroll
    for (int t = 0; t < 6; ++t) { const int row = 48 * wave + 8 * t + r; fma_row(acc, (part[row] + part[384 + row]) + (part[768 + row] + part[1152 + row]), wv[t]); }
#pragma unroll
    for (int k = 0; k < 24; ++k) { float x = acc[k]; x += dpp<ROR8>(x); x += __shfl_xor(x, 16); x += __shfl_xor(x, 32); acc[k] = x; }
    if (r == 0) {
#pragma unroll
      for (int i = 0; i < 3; ++i) {
        *reinterpret_cast<f32x4*>(red + wave * 192 + i * 64 + c * 8) = f32x4{acc[i * 8], acc[i * 8 + 1], acc[i * 8 + 2], acc[i * 8 + 3]};
        *reinterpret_cast<f32x4*>(red + wave * 192 + i * 64 + c * 8 + 4) = f32x4{acc[i * 8 + 4], acc[i * 8 + 5], acc[i * 8 + 6], acc[i * 8 + 7]};
      }
    }
    __syncthreads();
    if (tid < 192) {
      float s = 0.f;
#pragma unroll
      for (int w8 = 0; w8 < 8; ++w8) s += red[w8 * 192 + tid];
      st_granule(p.ws, WS_XG + ((h * 2 + half) * DH + tid) * 8, granule(s));
    }
  }
  STAMP(5);
  if (bb < 40) {
    float* buf = reinterpret_cast<float*>(lds + L_BIG);
    float* red = reinterpret_cast<float*>(lds + L_RED);
    __syncthreads();
    {
      const int j = tid >> 4, q = tid & 15;
      u32x4 v[6];
      for (unsigned spins = 0; spins < (1u << 22); ++spins) {
        bool ok = true;
#pragma unroll
        for (int i = 0; i < 6; ++i) { v[i] = ld16_dev_u(rs, WS_OG + ((bb * NA + j + 32 * i) * 32 + 2 * q) * 8); ok = ok && v[i][1] == 1u && v[i][3] == 1u; }
        if (__all(ok)) break;
        __builtin_amdgcn_s_sleep(4);
      }
#pragma unroll
      for (int i = 0; i < 6; ++i) { buf[(j + 32 * i) * 32 + 2 * q] = u2f(v[i][0]); buf[(j + 32 * i) * 32 + 2 * q + 1] = u2f(v[i][2]); }
    }
    STAMP(7);
    __syncthreads();
    if (tid < 256) {
      const int part = tid >> 5, col = tid & 31; float s = 0.f;
#pragma unroll
      for (int j = 0; j < 24; ++j) s += buf[(part * 24 + j) * 32 + col];
      red[tid] = s;
    }
    __syncthreads();
    if (tid < 32) {
      float s = 0.f;
#pragma unroll
      for (int j = 0; j < 8; ++j) s += red[j * 32 + tid];
      p.out[32 * bb + tid] = f2bf(s);
    }
  }
  STAMP(8);
}

__global__ void __launch_bounds__(NTHR, 2) fused_decode_kernel(Params p) {
  __shared__ __attribute__((aligned(16))) unsigned char lds[LDS_BYTES];
  const int b = blockIdx.x;
  STAMP(0);
  if (b < NA) {
    const int x = b & 7, idx = b >> 3;
    const int h = x * 4 + idx / ASPLIT, split = idx % ASPLIT;
    a_role(p, lds, h * ASPLIT + split, h, split);
  } else {
    const int bb = b - NA;
    b_role(p, lds, bb, (bb & 7) * 4 + (bb >> 4), (bb >> 3) & 1);
  }
}

extern "C" void kernel_launch(void* const* d_in, const int* in_sizes, int n_in,
                              void* d_out, int out_size, void* d_ws, size_t ws_size,
                              hipStream_t stream) {
  Params p;
  p.Ax = (const u16*)d_in[0]; p.Ak = (const u16*)d_in[1]; p.Av = (const u16*)d_in[2]; p.Awq = (const u16*)d_in[3]; p.Awo = (const u16*)d_in[4];
  p.Bx = (const u16*)d_in[5]; p.Bc = (const u16*)d_in[6]; p.Bwq = (const u16*)d_in[7]; p.Bwuk = (const u16*)d_in[8]; p.Bwuv = (const u16*)d_in[9]; p.Bwo = (const u16*)d_in[10];
  p.out = (u16*)d_out; p.ws = (unsigned char*)d_ws;
  (void)hipMemsetAsync((char*)d_ws + WS_CNT, 0, CNT_BYTES, stream);
  fused_decode_kernel<<<NA + NB, NTHR, 0, stream>>>(p);
}
```

```cpp
#include <hip/hip_runtime.h>
#include <hip/hip_bf16.h>
#include <stdint.h>

#define GAS __attribute__((address_space(1)))
#define LAS __attribute__((address_space(3)))
typedef unsigned u32x4 __attribute__((ext_vector_type(4)));
typedef unsigned u32x2 __attribute__((ext_vector_type(2)));
typedef float f32x4 __attribute__((ext_vector_type(4)));
typedef float f32x2 __attribute__((ext_vector_type(2)));
typedef short s16x8 __attribute__((ext_vector_type(8)));
typedef short s16x4 __attribute__((ext_vector_type(4)));
typedef __bf16 bf16x2_t __attribute__((ext_vector_type(2)));
typedef unsigned short u16;

constexpr int H = 32, SK = 8192, DH = 192, DM = 640, DC = 768, HD = H * DH;
constexpr int NA = 192, NB = 64, NTHR = 512, ASPLIT = 6;
#define RD_V 2
constexpr int RD = RD_V, RN = (22 + RD - 1) / RD;
constexpr float SCALE = 0.07216878364870322f;
constexpr float LOG2E = 1.4426950408889634f;

constexpr unsigned CNT_STRIDE = 128;
constexpr unsigned WS_CNT = 0;
constexpr unsigned WS_QG = 48 * CNT_STRIDE;
constexpr unsigned CNT_BYTES = WS_QG + 2 * 3072 * 8;
constexpr int C_ZB = 8;
constexpr int C_B1 = 9, C_B2 = 10;
constexpr unsigned WS_XG = 65536;
constexpr unsigned WS_PG = WS_XG + H * 2 * DH * 8;
constexpr unsigned WS_OG = WS_PG + NA * 200 * 8;
constexpr unsigned WS_QABS = WS_OG + 40 * NA * 32 * 8;
constexpr unsigned WS_BSTAT = WS_QABS + 2 * H * DC * 2;
constexpr unsigned WS_BCTX = WS_BSTAT + NB * H * 2 * 4;
constexpr unsigned WS_END = WS_BCTX + NB * H * DC * 4;
static_assert(CNT_BYTES <= WS_XG && CNT_BYTES % 16 == 0, "ws");
constexpr int ZPER = (int)((WS_QABS - WS_XG) / 16 / NB), ZPT = (ZPER + 511) / 512;
static_assert((WS_QABS - WS_XG) % (16 * NB) == 0, "ws");

constexpr int QROW = 1552;
constexpr int CROW = 784;
constexpr int PROW = 272;
constexpr int L_BIG = 0;
constexpr int L_P = 100352;
constexpr int L_ST = L_P + 32 * PROW;
constexpr int L_RED = L_ST + 2048;
constexpr int L_Q = L_RED + 16384;
constexpr int LDS_BYTES = L_Q + 1024;
constexpr int LA_WO = 0;
constexpr int LA_WOB = 40960;
constexpr int LA_RED = 81920;
constexpr int LA_Q = LA_RED + 24576;
static_assert(LA_Q + 4096 <= LDS_BYTES, "lds");

#define STAMP(i) do { } while (0)
struct Params {
  const u16* Ax; const u16* Ak; const u16* Av; const u16* Awq; const u16* Awo;
  const u16* Bx; const u16* Bc; const u16* Bwq; const u16* Bwuk; const u16* Bwuv; const u16* Bwo;
  u16* out; unsigned char* ws;
};

__device__ __forceinline__ unsigned f2u(float x) { return __builtin_bit_cast(unsigned, x); }
__device__ __forceinline__ float u2f(unsigned x) { return __builtin_bit_cast(float, x); }
__device__ __forceinline__ float bf_lo(unsigned u) { return __builtin_bit_cast(float, u << 16); }
__device__ __forceinline__ float bf_hi(unsigned u) { return __builtin_bit_cast(float, u & 0xffff0000u); }
__device__ __forceinline__ float bf2f(u16 u) { return __builtin_bit_cast(float, (unsigned)u << 16); }
__device__ __forceinline__ u16 f2bf(float x) { __bf16 b = (__bf16)x; return __builtin_bit_cast(u16, b); }
__device__ __forceinline__ float bf16r(float x) { return bf2f(f2bf(x)); }
__device__ __forceinline__ float dot2(unsigned a, unsigned b, float acc) {
  return __builtin_amdgcn_fdot2_f32_bf16(__builtin_bit_cast(bf16x2_t, a), __builtin_bit_cast(bf16x2_t, b), acc, false);
}
template <int CTRL> __device__ __forceinline__ float dpp(float x) {
  return __builtin_bit_cast(float, __builtin_amdgcn_mov_dpp(__builtin_bit_cast(int, x), CTRL, 0xf, 0xf, true));
}
constexpr int XOR1 = 0xB1, XOR2 = 0x4E, HMIRROR = 0x141, ROR8 = 0x128, ROR4 = 0x124;
__device__ __forceinline__ float sum8(float s) {
  s += dpp<XOR1>(s); s += dpp<XOR2>(s); s += dpp<HMIRROR>(s); return s;
}
__device__ __forceinline__ u32x4 ld16(const u16* p) { return __builtin_nontemporal_load(reinterpret_cast<const u32x4*>(p)); }

__device__ __forceinline__ __amdgpu_buffer_rsrc_t ws_rsrc(unsigned char* ws) {
  return __builtin_amdgcn_make_buffer_rsrc(ws, 0, (int)WS_END, 0x00020000);
}
__device__ __forceinline__ void st16_dev(__amdgpu_buffer_rsrc_t r, unsigned off, f32x4 v) {
  __builtin_amdgcn_raw_buffer_store_b128(__builtin_bit_cast(u32x4, v), r, off, 0, 16);
}
__device__ __forceinline__ void st16_dev_u(__amdgpu_buffer_rsrc_t r, unsigned off, u32x4 v) {
  __builtin_amdgcn_raw_buffer_store_b128(v, r, off, 0, 16);
}
__device__ __forceinline__ f32x4 ld16_dev(__amdgpu_buffer_rsrc_t r, unsigned off) {
  return __builtin_bit_cast(f32x4, __builtin_amdgcn_raw_buffer_load_b128(r, off, 0, 16));
}
__device__ __forceinline__ u32x4 ld16_dev_u(__amdgpu_buffer_rsrc_t r, unsigned off) {
  return __builtin_amdgcn_raw_buffer_load_b128(r, off, 0, 16);
}
__device__ __forceinline__ void st4_dev(unsigned char* ws, unsigned off, float v) {
  __hip_atomic_store((GAS float*)(ws + off), v, __ATOMIC_RELAXED, __HIP_MEMORY_SCOPE_AGENT);
}
__device__ __forceinline__ void st4_dev_u(unsigned char* ws, unsigned off, unsigned v) {
  __hip_atomic_store((GAS unsigned*)(ws + off), v, __ATOMIC_RELAXED, __HIP_MEMORY_SCOPE_AGENT);
}
__device__ __forceinline__ float ld4_dev(unsigned char* ws, unsigned off) {
  return __hip_atomic_load((GAS float*)(ws + off), __ATOMIC_RELAXED, __HIP_MEMORY_SCOPE_AGENT);
}
__device__ __forceinline__ void st8_dev(unsigned char* ws, unsigned off, float a, float b) {
  f32x2 v = {a, b};
  __hip_atomic_store((GAS unsigned long long*)(ws + off), __builtin_bit_cast(unsigned long long, v), __ATOMIC_RELAXED, __HIP_MEMORY_SCOPE_AGENT);
}
__device__ __forceinline__ f32x2 ld8_dev(unsigned char* ws, unsigned off) {
  unsigned long long v = __hip_atomic_load((GAS unsigned long long*)(ws + off), __ATOMIC_RELAXED, __HIP_MEMORY_SCOPE_AGENT);
  return __builtin_bit_cast(f32x2, v);
}
__device__ __forceinline__ void publish(unsigned char* ws, int cnt) {
  asm volatile("s_waitcnt vmcnt(0)" ::: "memory");
  __syncthreads();
  if (threadIdx.x == 0)
    __hip_atomic_fetch_add((GAS unsigned*)(ws + WS_CNT + cnt * CNT_STRIDE), 1u, __ATOMIC_RELAXED, __HIP_MEMORY_SCOPE_AGENT);
}
__device__ __forceinline__ void wait_counters(unsigned char* ws, int cnt, int n, unsigned want) {
  if (threadIdx.x < 64) {
    const int lane = threadIdx.x;
    GAS unsigned* p = (GAS unsigned*)(ws + WS_CNT + (cnt + (lane < n ? lane : 0)) * CNT_STRIDE);
    for (unsigned spins = 0; spins < (1u << 22); ++spins) {
      const unsigned v = __hip_atomic_load(p, __ATOMIC_RELAXED, __HIP_MEMORY_SCOPE_AGENT);
      if (__all(v >= want)) break;
      __builtin_amdgcn_s_sleep(2);
    }
  }
  __syncthreads();
}

__device__ __forceinline__ unsigned long long granule(float v) { return (1ull << 32) | (unsigned long long)__builtin_bit_cast(unsigned, v); }
__device__ __forceinline__ void st_granule(unsigned char* ws, unsigned off, unsigned long long gv) {
  __hip_atomic_store((GAS unsigned long long*)(ws + off), gv, __ATOMIC_RELAXED, __HIP_MEMORY_SCOPE_AGENT);
}
__device__ __forceinline__ unsigned long long ld_granule(unsigned char* ws, unsigned off) {
  return __hip_atomic_load((const GAS unsigned long long*)(ws + off), __ATOMIC_RELAXED, __HIP_MEMORY_SCOPE_AGENT);
}
__device__ __forceinline__ void gather_q(unsigned char* ws, int which, int h, unsigned* lq, unsigned (&qpk)[12]) {
  const int tid = threadIdx.x, c = tid & 7;
  if (tid < 64) {
    const unsigned goff = WS_QG + (which * 3072 + h * 96 + 2 * (tid < 48 ? tid : 0)) * 8;
    unsigned long long a = 0, b = 0;
    for (unsigned spins = 0; spins < (1u << 22); ++spins) {
      a = ld_granule(ws, goff);
      b = ld_granule(ws, goff + 8);
      if (__all((a >> 32) == 1ull && (b >> 32) == 1ull)) break;
      __builtin_amdgcn_s_sleep(2);
    }
    if (tid < 48) { lq[2 * tid] = (unsigned)a; lq[2 * tid + 1] = (unsigned)b; }
  }
  __syncthreads();
#pragma unroll
  for (int i = 0; i < 3; ++i)
#pragma unroll
    for (int k = 0; k < 4; ++k) qpk[i * 4 + k] = lq[i * 32 + c * 4 + k];
}
__device__ __forceinline__ unsigned zero_cnt_load(unsigned char* ws) {
  return __hip_atomic_load((const GAS unsigned*)(ws + WS_CNT + C_ZB * CNT_STRIDE), __ATOMIC_RELAXED, __HIP_MEMORY_SCOPE_AGENT);
}
__device__ __forceinline__ void zero_cnt_wait(unsigned char* ws, unsigned z) {
  for (unsigned spins = 0; spins < (1u << 22); ++spins) {
    if (__all(z >= (unsigned)NB)) break;
    __builtin_amdgcn_s_sleep(2);
    z = zero_cnt_load(ws);
  }
}

__device__ __forceinline__ void fma_row(float (&acc)[24], float s, const u32x4 (&w)[3]) {
#pragma unroll
  for (int i = 0; i < 3; ++i)
#pragma unroll
    for (int k = 0; k < 4; ++k) {
      acc[i * 8 + 2 * k] = fmaf(s, bf_lo(w[i][k]), acc[i * 8 + 2 * k]);
      acc[i * 8 + 2 * k + 1] = fmaf(s, bf_hi(w[i][k]), acc[i * 8 + 2 * k + 1]);
    }
}
__device__ __forceinline__ float dot_row(const u32x4 (&w)[3], const unsigned (&q)[12]) {
  float s0 = 0.f, s1 = 0.f, s2 = 0.f;
#pragma unroll
  for (int k = 0; k < 4; ++k) { s0 = dot2(w[0][k], q[k], s0); s1 = dot2(w[1][k], q[4 + k], s1); s2 = dot2(w[2][k], q[8 + k], s2); }
  return (s0 + s1) + s2;
}
__device__ __forceinline__ void a_role(const Params& p, unsigned char* lds, int wg, int h, int split) {
  const int tid = threadIdx.x, wave = __builtin_amdgcn_readfirstlane(tid >> 6), lane = tid & 63, r = lane >> 3, c = lane & 7;
  const int w0 = (1024 * split) / ASPLIT, w1 = (1024 * (split + 1)) / ASPLIT;
  const int t0 = w0 + wave, nt = (w1 - w0 - wave + 7) / 8;
  const u16* kb = p.Ak + ((size_t)h * SK + r) * DH + c * 8;
  const u16* vb = p.Av + ((size_t)h * SK + r) * DH + c * 8;
  const __amdgpu_buffer_rsrc_t rs = ws_rsrc(p.ws);
  u32x4 kr[RD][3], vr[RD][3];
  unsigned qpk[12];
  float* red = reinterpret_cast<float*>(lds + LA_RED);
#define A_LOAD(slot, idx) do { const int tt_ = t0 + 8 * ((idx) < nt ? (idx) : nt - 1); const size_t o_ = (size_t)tt_ * 8 * DH; \
    _Pragma("unroll") for (int i_ = 0; i_ < 3; ++i_) kr[slot][i_] = ld16(kb + o_ + i_ * 64); \
    _Pragma("unroll") for (int i_ = 0; i_ < 3; ++i_) vr[slot][i_] = ld16(vb + o_ + i_ * 64); } while (0)
  {
    const int which = split >= 3 ? 1 : 0, sl = split - 3 * which;
    const u16* x = which ? p.Bx : p.Ax;
    const u16* W = (which ? p.Bwq : p.Awq) + h * DH + 64 * sl + c * 8;
    float xa[10]; u32x4 wa[10];
#pragma unroll
    for (int t = 0; t < 10; ++t) xa[t] = bf2f(x[wave * 80 + t * 8 + r]);
#pragma unroll
    for (int t = 0; t < 10; ++t) wa[t] = ld16(W + (size_t)(wave * 80 + t * 8 + r) * HD);
#pragma unroll
    for (int d = 0; d < RD; ++d) A_LOAD(d, d);
    float a[8];
#pragma unroll
    for (int k = 0; k < 8; ++k) a[k] = 0.f;
#pragma unroll
    for (int t = 0; t < 10; ++t)
#pragma unroll
      for (int k = 0; k < 4; ++k) { a[2 * k] = fmaf(xa[t], bf_lo(wa[t][k]), a[2 * k]); a[2 * k + 1] = fmaf(xa[t], bf_hi(wa[t][k]), a[2 * k + 1]); }
#pragma unroll
    for (int k = 0; k < 8; ++k) { float v = a[k]; v += dpp<ROR8>(v); v += __shfl_xor(v, 16); v += __shfl_xor(v, 32); a[k] = v; }
    if (r == 0) {
      *reinterpret_cast<f32x4*>(red + wave * 64 + c * 8) = f32x4{a[0], a[1], a[2], a[3]};
      *reinterpret_cast<f32x4*>(red + wave * 64 + c * 8 + 4) = f32x4{a[4], a[5], a[6], a[7]};
    }
    __syncthreads();
    if (tid < 32) {
      float s0 = 0.f, s1 = 0.f;
#pragma unroll
      for (int w8 = 0; w8 < 8; ++w8) { s0 += red[w8 * 64 + 2 * tid]; s1 += red[w8 * 64 + 2 * tid + 1]; }
      const unsigned long long gv = (1ull << 32) | (unsigned long long)((unsigned)f2bf(s0) | ((unsigned)f2bf(s1) << 16));
      __hip_atomic_store((GAS unsigned long long*)(p.ws + WS_QG + (which * 3072 + h * 96 + 32 * sl + tid) * 8), gv, __ATOMIC_RELAXED, __HIP_MEMORY_SCOPE_AGENT);
    }
  }
  {
    u32x4 t[10];
    const u16* wo = p.Awo + (size_t)(h * DH + 32 * split) * DM;
    const u16* wob = p.Bwo + (size_t)(h * DH + 32 * split) * DM;
#pragma unroll
    for (int i = 0; i < 5; ++i) { t[i] = ld16(wo + (tid + 512 * i) * 8); t[5 + i] = ld16(wob + (tid + 512 * i) * 8); }
#pragma unroll
    for (int i = 0; i < 5; ++i) {
      *reinterpret_cast<u32x4*>(lds + LA_WO + (tid + 512 * i) * 16) = t[i];
      *reinterpret_cast<u32x4*>(lds + LA_WOB + (tid + 512 * i) * 16) = t[5 + i];
    }
  }
  gather_q(p.ws, 0, h, reinterpret_cast<unsigned*>(lds + LA_Q), qpk);
  STAMP(1);
  float m = -INFINITY, l = 0.f, acc[24];
#pragma unroll
  for (int k = 0; k < 24; ++k) acc[k] = 0.f;
#define A_STEP(slot, idx) do { \
    float s_ = sum8(dot_row(kr[slot], qpk)); \
    s_ = bf16r(bf16r(s_) * SCALE); \
    s_ = ((idx) < nt) ? s_ : -INFINITY; \
    const float mn_ = fmaxf(m, s_); \
    const float al_ = __builtin_amdgcn_exp2f((m - mn_) * LOG2E), pp_ = __builtin_amdgcn_exp2f((s_ - mn_) * LOG2E); \
    l = fmaf(l, al_, pp_); m = mn_; \
    _Pragma("unroll") for (int k_ = 0; k_ < 24; ++k_) acc[k_] *= al_; \
    fma_row(acc, pp_, vr[slot]); } while (0)
  for (int it = 0; it < RN - 1; ++it) {
#pragma unroll
    for (int d = 0; d < RD; ++d) { A_STEP(d, it * RD + d); A_LOAD(d, it * RD + d + RD); }
  }
  const unsigned zc = zero_cnt_load(p.ws);
#pragma unroll
  for (int d = 0; d < RD; ++d) A_STEP(d, (RN - 1) * RD + d);
  zero_cnt_wait(p.ws, zc);
#undef A_STEP
#undef A_LOAD
  STAMP(2);
  {
    float M = m; M = fmaxf(M, dpp<ROR8>(M)); M = fmaxf(M, __shfl_xor(M, 16)); M = fmaxf(M, __shfl_xor(M, 32));
    const float f = __builtin_amdgcn_exp2f((m - M) * LOG2E);
    l *= f; l += dpp<ROR8>(l); l += __shfl_xor(l, 16); l += __shfl_xor(l, 32);
#pragma unroll
    for (int k = 0; k < 24; ++k) { float v = acc[k] * f; v += dpp<ROR8>(v); v += __shfl_xor(v, 16); v += __shfl_xor(v, 32); acc[k] = v; }
    if (r == 0) {
#pragma unroll
      for (int i = 0; i < 3; ++i) {
        *reinterpret_cast<f32x4*>(red + wave * 200 + i * 64 + c * 8) = f32x4{acc[i * 8], acc[i * 8 + 1], acc[i * 8 + 2], acc[i * 8 + 3]};
        *reinterpret_cast<f32x4*>(red + wave * 200 + i * 64 + c * 8 + 4) = f32x4{acc[i * 8 + 4], acc[i * 8 + 5], acc[i * 8 + 6], acc[i * 8 + 7]};
      }
      if (c == 0) { red[wave * 200 + 192] = M; red[wave * 200 + 193] = l; }
    }
    __syncthreads();
    if (tid < 194) {
      float MM = -INFINITY;
#pragma unroll
      for (int w8 = 0; w8 < 8; ++w8) MM = fmaxf(MM, red[w8 * 200 + 192]);
      float o = 0.f;
      const int src = tid < 192 ? tid : 193;
#pragma unroll
      for (int w8 = 0; w8 < 8; ++w8) o = fmaf(__builtin_amdgcn_exp2f((red[w8 * 200 + 192] - MM) * LOG2E), red[w8 * 200 + src], o);
      st_granule(p.ws, WS_PG + (wg * 200 + tid) * 8, granule(tid == 192 ? MM : o));
    }
  }
  STAMP(3);
  float* gat = reinterpret_cast<float*>(lds + LA_Q);
  float* sv = gat + 512;
  if (tid < 320) {
    unsigned off;
    if (tid < 256) { const int t_ = tid < 204 ? tid : 0, j = t_ / 34, k = t_ % 34; off = WS_PG + ((h * ASPLIT + j) * 200 + (k < 32 ? 32 * split + k : 160 + k)) * 8; }
    else off = WS_XG + ((h * 2 + ((tid >> 5) & 1)) * DH + 32 * split + (tid & 31)) * 8;
    unsigned long long a = 0;
    for (unsigned spins = 0; spins < (1u << 22); ++spins) {
      a = ld_granule(p.ws, off);
      if (__all((a >> 32) == 1ull)) break;
      __builtin_amdgcn_s_sleep(1);
    }
    gat[tid] = __builtin_bit_cast(float, (unsigned)a);
  }
  __syncthreads();
  STAMP(5);
  if (tid < 32) {
    float MM = -INFINITY;
#pragma unroll
    for (int j = 0; j < ASPLIT; ++j) MM = fmaxf(MM, gat[j * 34 + 32]);
    float o = 0.f, L = 0.f;
#pragma unroll
    for (int j = 0; j < ASPLIT; ++j) { const float wt = __builtin_amdgcn_exp2f((gat[j * 34 + 32] - MM) * LOG2E); o = fmaf(wt, gat[j * 34 + tid], o); L = fmaf(wt, gat[j * 34 + 33], L); }
    sv[tid] = bf16r(o / L);
  } else if (tid < 64) {
    sv[tid] = bf16r(gat[256 + tid - 32] + gat[256 + tid]);
  }
  __syncthreads();
  {
    float* red2 = red;
    const int chunk = tid % 80, rg = tid / 80;
    if (tid < 320) {
      float a[8], b[8];
#pragma unroll
      for (int k = 0; k < 8; ++k) { a[k] = 0.f; b[k] = 0.f; }
#pragma unroll
      for (int i = 0; i < 8; ++i) {
        const float sa = sv[rg * 8 + i], sb = sv[32 + rg * 8 + i];
        const u32x4 wa = *reinterpret_cast<const u32x4*>(lds + LA_WO + (rg * 8 + i) * 1280 + chunk * 16);
        const u32x4 wb = *reinterpret_cast<const u32x4*>(lds + LA_WOB + (rg * 8 + i) * 1280 + chunk * 16);
#pragma unroll
        for (int k = 0; k < 4; ++k) {
          a[2 * k] = fmaf(sa, bf_lo(wa[k]), a[2 * k]); a[2 * k + 1] = fmaf(sa, bf_hi(wa[k]), a[2 * k + 1]);
          b[2 * k] = fmaf(sb, bf_lo(wb[k]), b[2 * k]); b[2 * k + 1] = fmaf(sb, bf_hi(wb[k]), b[2 * k + 1]);
        }
      }
      *reinterpret_cast<f32x4*>(red2 + rg * 640 + chunk * 8) = f32x4{a[0], a[1], a[2], a[3]};
      *reinterpret_cast<f32x4*>(red2 + rg * 640 + chunk * 8 + 4) = f32x4{a[4], a[5], a[6], a[7]};
      *reinterpret_cast<f32x4*>(red2 + 2560 + rg * 640 + chunk * 8) = f32x4{b[0], b[1], b[2], b[3]};
      *reinterpret_cast<f32x4*>(red2 + 2560 + rg * 640 + chunk * 8 + 4) = f32x4{b[4], b[5], b[6], b[7]};
    }
    __syncthreads();
    if (tid < 320) {
      const float* src = red2 + (tid < 160 ? tid * 4 : 2560 + (tid - 160) * 4);
      f32x4 sum = *reinterpret_cast<const f32x4*>(src);
#pragma unroll
      for (int g = 1; g < 4; ++g) sum += *reinterpret_cast<const f32x4*>(src + g * 640);
      const unsigned off = WS_OG + (((tid >> 3) * NA + wg) * 32 + (tid & 7) * 4) * 8;
      st16_dev_u(rs, off, u32x4{f2u(sum[0]), 1u, f2u(sum[1]), 1u});
      st16_dev_u(rs, off + 16, u32x4{f2u(sum[2]), 1u, f2u(sum[3]), 1u});
    }
  }
  STAMP(6);
}

__device__ __forceinline__ void b_role(const Params& p, unsigned char* lds, int bb, int h, int half) {
  const int tid = threadIdx.x, wave = tid >> 6, lane = tid & 63, r = lane >> 3, c = lane & 7, r16 = lane & 15, g = lane >> 4;
  const __amdgpu_buffer_rsrc_t rs = ws_rsrc(p.ws);
  u32x4 cf[24];
  {
#pragma unroll
    for (int i = 0; i < ZPT; ++i) { const unsigned o = (unsigned)(bb * ZPER + tid + 512 * i); if (tid + 512 * i < ZPER) st16_dev_u(rs, WS_XG + o * 16, u32x4{0u, 0u, 0u, 0u}); }
    asm volatile("" ::: "memory");
    u32x4 w[6][3];
#pragma unroll
    for (int t = 0; t < 6; ++t) {
      const u16* q_ = p.Bwuk + (size_t)(384 * half + 48 * wave + 8 * t + r) * HD + h * DH + c * 8;
#pragma unroll
      for (int i = 0; i < 3; ++i) w[t][i] = ld16(q_ + i * 64);
    }
    asm volatile("s_waitcnt vmcnt(18)" ::: "memory");
    __syncthreads();
    if (tid == 64) __hip_atomic_fetch_add((GAS unsigned*)(p.ws + WS_CNT + C_ZB * CNT_STRIDE), 1u, __ATOMIC_RELAXED, __HIP_MEMORY_SCOPE_AGENT);
    unsigned qpk[12];
    gather_q(p.ws, 1, h, reinterpret_cast<unsigned*>(lds + L_Q), qpk);
    u16* sh = reinterpret_cast<u16*>(lds + L_RED);
#pragma unroll
    for (int t = 0; t < 6; ++t) {
      const float s = sum8(dot_row(w[t], qpk));
      const u16 hi = f2bf(s); const u16 lo = f2bf(s - bf2f(hi));
      if (c == 0) { sh[48 * wave + 8 * t + r] = hi; sh[384 + 48 * wave + 8 * t + r] = lo; }
    }
    __syncthreads();
    if (tid < 96) {
      const int part = tid / 48, ch = tid % 48;
      const u32x4 v = *reinterpret_cast<const u32x4*>(sh + part * 384 + ch * 8);
      st16_dev_u(rs, WS_QABS + ((part * H + h) * DC + 384 * half + ch * 8) * 2, v);
    }
    publish(p.ws, C_B1);
  }
  STAMP(1);
  {
    u32x4 raw[24];
    const u16* cp = p.Bc + (size_t)(128 * bb + 16 * wave + r) * DC + 8 * c;
#pragma unroll
    for (int hf = 0; hf < 2; ++hf)
#pragma unroll
      for (int rg = 0; rg < 2; ++rg)
#pragma unroll
        for (int i = 0; i < 6; ++i) raw[hf * 12 + rg * 6 + i] = ld16(cp + (size_t)(8 * rg) * DC + 384 * hf + 64 * i);
#pragma unroll
    for (int hf = 0; hf < 2; ++hf) {
#pragma unroll
      for (int rg = 0; rg < 2; ++rg)
#pragma unroll
        for (int i = 0; i < 6; ++i)
          *reinterpret_cast<u32x4*>(lds + L_BIG + (16 * wave + 8 * rg + r) * CROW + (64 * i + 8 * c) * 2) = raw[hf * 12 + rg * 6 + i];
      asm volatile("s_waitcnt lgkmcnt(0)" ::: "memory");
#pragma unroll
      for (int s = 0; s < 12; ++s) cf[12 * hf + s] = *reinterpret_cast<const u32x4*>(lds + L_BIG + (16 * wave + r16) * CROW + (32 * s + 8 * g) * 2);
      asm volatile("s_waitcnt lgkmcnt(0)" ::: "memory");
    }
  }
  wait_counters(p.ws, C_B1, 1, NB);
  STAMP(2);
#pragma unroll
  for (int i = 0; i < 12; ++i) {
    const int q = tid + 512 * i;
    const int row = q / 96, ch = q % 96;
    const u32x4 v = ld16_dev_u(rs, WS_QABS + q * 16);
    *reinterpret_cast<u32x4*>(lds + L_BIG + row * QROW + ch * 16) = v;
  }
  u32x4 wv[6][3];
#pragma unroll
  for (int t = 0; t < 6; ++t) {
    const u16* q_ = p.Bwuv + (size_t)(384 * half + 48 * wave + 8 * t + r) * HD + h * DH + c * 8;
#pragma unroll
    for (int i = 0; i < 3; ++i) wv[t][i] = ld16(q_ + i * 64);
  }
  __syncthreads();
  f32x4 sc[2] = {f32x4{0.f, 0.f, 0.f, 0.f}, f32x4{0.f, 0.f, 0.f, 0.f}};
#pragma unroll
  for (int s = 0; s < 24; ++s) {
#pragma unroll
    for (int ht = 0; ht < 2; ++ht) {
      const s16x8 bh = *reinterpret_cast<const s16x8*>(lds + L_BIG + (16 * ht + r16) * QROW + (32 * s + 8 * g) * 2);
      const s16x8 bl = *reinterpret_cast<const s16x8*>(lds + L_BIG + (32 + 16 * ht + r16) * QROW + (32 * s + 8 * g) * 2);
      sc[ht] = __builtin_amdgcn_mfma_f32_16x16x32_bf16(__builtin_bit_cast(s16x8, cf[s]), bh, sc[ht], 0, 0, 0);
      sc[ht] = __builtin_amdgcn_mfma_f32_16x16x32_bf16(__builtin_bit_cast(s16x8, cf[s]), bl, sc[ht], 0, 0, 0);
    }
  }
  float* wmax = reinterpret_cast<float*>(lds + L_ST);
  float* wsum = wmax + 256;
#pragma unroll
  for (int ht = 0; ht < 2; ++ht) {
#pragma unroll
    for (int k = 0; k < 4; ++k) sc[ht][k] = bf16r(bf16r(sc[ht][k]) * SCALE);
    float mx = fmaxf(fmaxf(sc[ht][0], sc[ht][1]), fmaxf(sc[ht][2], sc[ht][3]));
    mx = fmaxf(mx, __shfl_xor(mx, 16)); mx = fmaxf(mx, __shfl_xor(mx, 32));
    if (g == 0) wmax[wave * 32 + 16 * ht + r16] = mx;
  }
  __syncthreads();
#pragma unroll
  for (int ht = 0; ht < 2; ++ht) {
    float M = -INFINITY;
#pragma unroll
    for (int w8 = 0; w8 < 8; ++w8) M = fmaxf(M, wmax[w8 * 32 + 16 * ht + r16]);
    float e[4], sum = 0.f;
#pragma unroll
    for (int k = 0; k < 4; ++k) { e[k] = __builtin_amdgcn_exp2f((sc[ht][k] - M) * LOG2E); sum += e[k]; }
    sum += __shfl_xor(sum, 16); sum += __shfl_xor(sum, 32);
    if (g == 0) wsum[wave * 32 + 16 * ht + r16] = sum;
    u32x2 pk = {(unsigned)f2bf(e[0]) | ((unsigned)f2bf(e[1]) << 16), (unsigned)f2bf(e[2]) | ((unsigned)f2bf(e[3]) << 16)};
    *reinterpret_cast<u32x2*>(lds + L_P + (16 * ht + r16) * PROW + (16 * wave + 4 * g) * 2) = pk;
  }
#pragma unroll
  for (int s = 0; s < 12; ++s) *reinterpret_cast<u32x4*>(lds + L_BIG + (16 * wave + r16) * CROW + (32 * s + 8 * g) * 2) = cf[s];
  __syncthreads();
  if (tid < 32) {
    float M = -INFINITY, L = 0.f;
#pragma unroll
    for (int w8 = 0; w8 < 8; ++w8) { M = fmaxf(M, wmax[w8 * 32 + tid]); L += wsum[w8 * 32 + tid]; }
    st8_dev(p.ws, WS_BSTAT + (bb * H + tid) * 8, M, L);
  }
  {
    s16x8 pf[2][4];
#pragma unroll
    for (int ht = 0; ht < 2; ++ht)
#pragma unroll
      for (int ks = 0; ks < 4; ++ks) pf[ht][ks] = *reinterpret_cast<const s16x8*>(lds + L_P + (16 * ht + r16) * PROW + (32 * ks + 8 * g) * 2);
    const int q4 = r16 >> 2, p4 = r16 & 3;
#pragma unroll
    for (int ch = 0; ch < 2; ++ch) {
      if (ch == 1) {
        __syncthreads();
#pragma unroll
        for (int s = 0; s < 12; ++s) *reinterpret_cast<u32x4*>(lds + L_BIG + (16 * wave + r16) * CROW + (32 * s + 8 * g) * 2) = cf[12 + s];
        __syncthreads();
      }
#pragma unroll
      for (int ct = 0; ct < 3; ++ct) {
        f32x4 d[2] = {f32x4{0.f, 0.f, 0.f, 0.f}, f32x4{0.f, 0.f, 0.f, 0.f}};
#pragma unroll
        for (int ks = 0; ks < 4; ++ks) {
          const unsigned char* a0 = lds + L_BIG + (32 * ks + 8 * g + q4) * CROW + (48 * wave + 16 * ct + 4 * p4) * 2;
          typedef short v4i16_t __attribute__((ext_vector_type(4)));
          const v4i16_t t0 = __builtin_amdgcn_ds_read_tr16_b64_v4i16((LAS v4i16_t*)(a0));
          const v4i16_t t1 = __builtin_amdgcn_ds_read_tr16_b64_v4i16((LAS v4i16_t*)(a0 + 4 * CROW));
          const s16x8 af = {t0[0], t0[1], t0[2], t0[3], t1[0], t1[1], t1[2], t1[3]};
#pragma unroll
          for (int ht = 0; ht < 2; ++ht) d[ht] = __builtin_amdgcn_mfma_f32_16x16x32_bf16(af, pf[ht][ks], d[ht], 0, 0, 0);
        }
#pragma unroll
        for (int ht = 0; ht < 2; ++ht)
          st16_dev(rs, WS_BCTX + (unsigned)(((bb * H + 16 * ht + r16) * DC + 384 * ch + 48 * wave + 16 * ct + 4 * g) * 4), d[ht]);
      }
    }
  }
  publish(p.ws, C_B2);
  STAMP(3);
  zero_cnt_wait(p.ws, zero_cnt_load(p.ws));
  wait_counters(p.ws, C_B2, 1, NB);
  STAMP(4);
  {
    float* coef = reinterpret_cast<float*>(lds + L_ST);
    float* part = reinterpret_cast<float*>(lds + L_BIG);
    float* red = reinterpret_cast<float*>(lds + L_RED);
    const int jg = tid / 96, q = tid % 96;
    f32x4 v[16];
    if (tid < 384) {
#pragma unroll
      for (int j = 0; j < 16; ++j) v[j] = ld16_dev(rs, WS_BCTX + (unsigned)((((16 * jg + j) * H + h) * DC + 384 * half + 4 * q) * 4));
    }
    if (tid >= 448) {
      const int j = tid - 448;
      const f32x2 ml = ld8_dev(p.ws, WS_BSTAT + (j * H + h) * 8);
      float M = ml[0];
#pragma unroll
      for (int o = 1; o < 64; o <<= 1) M = fmaxf(M, __shfl_xor(M, o));
      const float wt = __builtin_amdgcn_exp2f((ml[0] - M) * LOG2E);
      float L = wt * ml[1];
#pragma unroll
      for (int o = 1; o < 64; o <<= 1) L += __shfl_xor(L, o);
      coef[j] = wt / L;
    }
    __syncthreads();
    if (tid < 384) {
      f32x4 a = {0.f, 0.f, 0.f, 0.f};
#pragma unroll
      for (int j = 0; j < 16; ++j) a += coef[16 * jg + j] * v[j];
      *reinterpret_cast<f32x4*>(part + jg * 384 + 4 * q) = a;
    }
    __syncthreads();
    float acc[24];
#pragma unroll
    for (int k = 0; k < 24; ++k) acc[k] = 0.f;
#pragma unroll
    for (int t = 0; t < 6; ++t) { const int row = 48 * wave + 8 * t + r; fma_row(acc, (part[row] + part[384 + row]) + (part[768 + row] + part[1152 + row]), wv[t]); }
#pragma unroll
    for (int k = 0; k < 24; ++k) { float x = acc[k]; x += dpp<ROR8>(x); x += __shfl_xor(x, 16); x += __shfl_xor(x, 32); acc[k] = x; }
    if (r == 0) {
#pragma unroll
      for (int i = 0; i < 3; ++i) {
        *reinterpret_cast<f32x4*>(red + wave * 192 + i * 64 + c * 8) = f32x4{acc[i * 8], acc[i * 8 + 1], acc[i * 8 + 2], acc[i * 8 + 3]};
        *reinterpret_cast<f32x4*>(red + wave * 192 + i * 64 + c * 8 + 4) = f32x4{acc[i * 8 + 4], acc[i * 8 + 5], acc[i * 8 + 6], acc[i * 8 + 7]};
      }
    }
    __syncthreads();
    if (tid < 192) {
      float s = 0.f;
#pragma unroll
      for (int w8 = 0; w8 < 8; ++w8) s += red[w8 * 192 + tid];
      st_granule(p.ws, WS_XG + ((h * 2 + half) * DH + tid) * 8, granule(s));
    }
  }
  STAMP(5);
  if (bb < 40) {
    float* buf = reinterpret_cast<float*>(lds + L_BIG);
    float* red = reinterpret_cast<float*>(lds + L_RED);
    __syncthreads();
    {
      const int j = tid >> 4, q = tid & 15;
      u32x4 v[6];
      for (unsigned spins = 0; spins < (1u << 22); ++spins) {
        bool ok = true;
#pragma unroll
        for (int i = 0; i < 6; ++i) { v[i] = ld16_dev_u(rs, WS_OG + ((bb * NA + j + 32 * i) * 32 + 2 * q) * 8); ok = ok && v[i][1] == 1u && v[i][3] == 1u; }
        if (__all(ok)) break;
        __builtin_amdgcn_s_sleep(4);
      }
#pragma unroll
      for (int i = 0; i < 6; ++i) { buf[(j + 32 * i) * 32 + 2 * q] = u2f(v[i][0]); buf[(j + 32 * i) * 32 + 2 * q + 1] = u2f(v[i][2]); }
    }
    STAMP(7);
    __syncthreads();
    if (tid < 256) {
      const int part = tid >> 5, col = tid & 31; float s = 0.f;
#pragma unroll
      for (int j = 0; j < 24; ++j) s += buf[(part * 24 + j) * 32 + col];
      red[tid] = s;
    }
    __syncthreads();
    if (tid < 32) {
      float s = 0.f;
#pragma unroll
      for (int j = 0; j < 8; ++j) s += red[j * 32 + tid];
      p.out[32 * bb + tid] = f2bf(s);
    }
  }
  STAMP(8);
}

__global__ void __launch_bounds__(NTHR, 2) fused_decode_kernel(Params p) {
  __shared__ __attribute__((aligned(16))) unsigned char lds[LDS_BYTES];
  const int b = blockIdx.x;
  STAMP(0);
  if (b < NA) {
    const int x = b & 7, idx = b >> 3;
    const int h = x * 4 + idx / ASPLIT, split = idx % ASPLIT;
    a_role(p, lds, h * ASPLIT + split, h, split);
  } else {
    const int bb = b - NA;
    b_role(p, lds, bb, (bb & 7) * 4 + (bb >> 4), (bb >> 3) & 1);
  }
}

extern "C" void kernel_launch(void* const* d_in, const int* in_sizes, int n_in,
                              void* d_out, int out_size, void* d_ws, size_t ws_size,
                              hipStream_t stream) {
  Params p;
  p.Ax = (const u16*)d_in[0]; p.Ak = (const u16*)d_in[1]; p.Av = (const u16*)d_in[2]; p.Awq = (const u16*)d_in[3]; p.Awo = (const u16*)d_in[4];
  p.Bx = (const u16*)d_in[5]; p.Bc = (const u16*)d_in[6]; p.Bwq = (const u16*)d_in[7]; p.Bwuk = (const u16*)d_in[8]; p.Bwuv = (const u16*)d_in[9]; p.Bwo = (const u16*)d_in[10];
  p.out = (u16*)d_out; p.ws = (unsigned char*)d_ws;
  (void)hipMemsetAsync((char*)d_ws + WS_CNT, 0, CNT_BYTES, stream);
  fused_decode_kernel<<<NA + NB, NTHR, 0, stream>>>(p);
}
```

```cpp
#include <hip/hip_runtime.h>
#include <hip/hip_bf16.h>
#include <stdint.h>

#define GAS __attribute__((address_space(1)))
#define LAS __attribute__((address_space(3)))
typedef unsigned u32x4 __attribute__((ext_vector_type(4)));
typedef unsigned u32x2 __attribute__((ext_vector_type(2)));
typedef float f32x4 __attribute__((ext_vector_type(4)));
typedef float f32x2 __attribute__((ext_vector_type(2)));
typedef short s16x8 __attribute__((ext_vector_type(8)));
typedef short s16x4 __attribute__((ext_vector_type(4)));
typedef __bf16 bf16x2_t __attribute__((ext_vector_type(2)));
typedef unsigned short u16;

constexpr int H = 32, SK = 8192, DH = 192, DM = 640, DC = 768, HD = H * DH;
constexpr int NA = 192, NB = 64, NTHR = 512, ASPLIT = 6;
constexpr float SCALE = 0.07216878364870322f;
constexpr float LOG2E = 1.4426950408889634f;

constexpr unsigned WS_EPOCH = 0;
constexpr unsigned WS_FZ = 1024, WS_F1 = 2048, WS_F2 = 3072;
constexpr unsigned WS_QG = 4096;
constexpr unsigned WS_XG = 131072;
constexpr unsigned WS_PG = WS_XG + H * 2 * DH * 8;
constexpr unsigned WS_OG = WS_PG + NA * 200 * 8;
constexpr unsigned WS_QABS = WS_OG + 40 * NA * 32 * 8;
constexpr unsigned WS_BSTAT = WS_QABS + 2 * H * DC * 2;
constexpr unsigned WS_BCTX = WS_BSTAT + NB * H * 2 * 4;
constexpr unsigned WS_END = WS_BCTX + NB * H * DC * 4;
static_assert(WS_QG + 4 * 3072 * 8 <= WS_XG, "ws");
constexpr int ZPER = (int)((WS_QABS - WS_XG) / 16 / NB), ZPT = (ZPER + 511) / 512;
static_assert((WS_QABS - WS_XG) % (16 * NB) == 0, "ws");

constexpr int QROW = 1552;
constexpr int CROW = 784;
constexpr int PROW = 272;
constexpr int L_BIG = 0;
constexpr int L_P = 100352;
constexpr int L_ST = L_P + 32 * PROW;
constexpr int L_RED = L_ST + 2048;
constexpr int L_Q = L_RED + 16384;
constexpr int LDS_BYTES = L_Q + 1024;
constexpr int LA_WO = 0;
constexpr int LA_WOB = 40960;
constexpr int LA_RED = 81920;
constexpr int LA_Q = LA_RED + 24576;
static_assert(LA_Q + 4096 <= LDS_BYTES, "lds");

#define STAMP(i) do { } while (0)
struct Params {
  const u16* Ax; const u16* Ak; const u16* Av; const u16* Awq; const u16* Awo;
  const u16* Bx; const u16* Bc; const u16* Bwq; const u16* Bwuk; const u16* Bwuv; const u16* Bwo;
  u16* out; unsigned char* ws;
};

__device__ __forceinline__ unsigned f2u(float x) { return __builtin_bit_cast(unsigned, x); }
__device__ __forceinline__ float u2f(unsigned x) { return __builtin_bit_cast(float, x); }
__device__ __forceinline__ float bf_lo(unsigned u) { return __builtin_bit_cast(float, u << 16); }
__device__ __forceinline__ float bf_hi(unsigned u) { return __builtin_bit_cast(float, u & 0xffff0000u); }
__device__ __forceinline__ float bf2f(u16 u) { return __builtin_bit_cast(float, (unsigned)u << 16); }
__device__ __forceinline__ u16 f2bf(float x) { __bf16 b = (__bf16)x; return __builtin_bit_cast(u16, b); }
__device__ __forceinline__ float bf16r(float x) { return bf2f(f2bf(x)); }
__device__ __forceinline__ float dot2(unsigned a, unsigned b, float acc) {
  return __builtin_amdgcn_fdot2_f32_bf16(__builtin_bit_cast(bf16x2_t, a), __builtin_bit_cast(bf16x2_t, b), acc, false);
}
template <int CTRL> __device__ __forceinline__ float dpp(float x) {
  return __builtin_bit_cast(float, __builtin_amdgcn_mov_dpp(__builtin_bit_cast(int, x), CTRL, 0xf, 0xf, true));
}
constexpr int XOR1 = 0xB1, XOR2 = 0x4E, HMIRROR = 0x141, ROR8 = 0x128, ROR4 = 0x124;
__device__ __forceinline__ float sum8(float s) {
  s += dpp<XOR1>(s); s += dpp<XOR2>(s); s += dpp<HMIRROR>(s); return s;
}
__device__ __forceinline__ u32x4 ld16(const u16* p) { return __builtin_nontemporal_load(reinterpret_cast<const u32x4*>(p)); }

__device__ __forceinline__ __amdgpu_buffer_rsrc_t ws_rsrc(unsigned char* ws) {
  return __builtin_amdgcn_make_buffer_rsrc(ws, 0, (int)WS_END, 0x00020000);
}
__device__ __forceinline__ void st16_dev(__amdgpu_buffer_rsrc_t r, unsigned off, f32x4 v) {
  __builtin_amdgcn_raw_buffer_store_b128(__builtin_bit_cast(u32x4, v), r, off, 0, 16);
}
__device__ __forceinline__ void st16_dev_u(__amdgpu_buffer_rsrc_t r, unsigned off, u32x4 v) {
  __builtin_amdgcn_raw_buffer_store_b128(v, r, off, 0, 16);
}
__device__ __forceinline__ f32x4 ld16_dev(__amdgpu_buffer_rsrc_t r, unsigned off) {
  return __builtin_bit_cast(f32x4, __builtin_amdgcn_raw_buffer_load_b128(r, off, 0, 16));
}
__device__ __forceinline__ u32x4 ld16_dev_u(__amdgpu_buffer_rsrc_t r, unsigned off) {
  return __builtin_amdgcn_raw_buffer_load_b128(r, off, 0, 16);
}
__device__ __forceinline__ void st4_dev(unsigned char* ws, unsigned off, float v) {
  __hip_atomic_store((GAS float*)(ws + off), v, __ATOMIC_RELAXED, __HIP_MEMORY_SCOPE_AGENT);
}
__device__ __forceinline__ void st4_dev_u(unsigned char* ws, unsigned off, unsigned v) {
  __hip_atomic_store((GAS unsigned*)(ws + off), v, __ATOMIC_RELAXED, __HIP_MEMORY_SCOPE_AGENT);
}
__device__ __forceinline__ float ld4_dev(unsigned char* ws, unsigned off) {
  return __hip_atomic_load((GAS float*)(ws + off), __ATOMIC_RELAXED, __HIP_MEMORY_SCOPE_AGENT);
}
__device__ __forceinline__ void st8_dev(unsigned char* ws, unsigned off, float a, float b) {
  f32x2 v = {a, b};
  __hip_atomic_store((GAS unsigned long long*)(ws + off), __builtin_bit_cast(unsigned long long, v), __ATOMIC_RELAXED, __HIP_MEMORY_SCOPE_AGENT);
}
__device__ __forceinline__ f32x2 ld8_dev(unsigned char* ws, unsigned off) {
  unsigned long long v = __hip_atomic_load((GAS unsigned long long*)(ws + off), __ATOMIC_RELAXED, __HIP_MEMORY_SCOPE_AGENT);
  return __builtin_bit_cast(f32x2, v);
}
typedef unsigned long long u64;
__device__ __forceinline__ u64 call_tag(unsigned char* ws) {
  u64 z = __hip_atomic_load((const GAS u64*)(ws + WS_EPOCH), __ATOMIC_RELAXED, __HIP_MEMORY_SCOPE_AGENT) + 1ull;
  z *= 0x9E3779B97F4A7C15ull;
  z = (z ^ (z >> 30)) * 0xBF58476D1CE4E5B9ull; z = (z ^ (z >> 27)) * 0x94D049BB133111EBull;
  return z ^ (z >> 31);
}
__device__ __forceinline__ void set_flag(unsigned char* ws, unsigned base, int i, u64 T) {
  __hip_atomic_store((GAS u64*)(ws + base + i * 8), T, __ATOMIC_RELAXED, __HIP_MEMORY_SCOPE_AGENT);
}
__device__ __forceinline__ u64 flag_load(unsigned char* ws, unsigned base) {
  return __hip_atomic_load((const GAS u64*)(ws + base + (threadIdx.x & 63) * 8), __ATOMIC_RELAXED, __HIP_MEMORY_SCOPE_AGENT);
}
__device__ __forceinline__ void flag_wait(unsigned char* ws, unsigned base, u64 T, u64 z) {
  for (unsigned spins = 0; spins < (1u << 22); ++spins) {
    if (__all(z == T)) break;
    __builtin_amdgcn_s_sleep(2);
    z = flag_load(ws, base);
  }
}
__device__ __forceinline__ void publish(unsigned char* ws, unsigned base, int i, u64 T) {
  asm volatile("s_waitcnt vmcnt(0)" ::: "memory");
  __syncthreads();
  if (threadIdx.x == 0) set_flag(ws, base, i, T);
}
__device__ __forceinline__ void wait_flags(unsigned char* ws, unsigned base, u64 T) {
  if (threadIdx.x < 64) flag_wait(ws, base, T, flag_load(ws, base));
  __syncthreads();
}

__device__ __forceinline__ unsigned long long granule(float v) { return (1ull << 32) | (unsigned long long)__builtin_bit_cast(unsigned, v); }
__device__ __forceinline__ void st_granule(unsigned char* ws, unsigned off, unsigned long long gv) {
  __hip_atomic_store((GAS unsigned long long*)(ws + off), gv, __ATOMIC_RELAXED, __HIP_MEMORY_SCOPE_AGENT);
}
__device__ __forceinline__ unsigned long long ld_granule(unsigned char* ws, unsigned off) {
  return __hip_atomic_load((const GAS unsigned long long*)(ws + off), __ATOMIC_RELAXED, __HIP_MEMORY_SCOPE_AGENT);
}
__device__ __forceinline__ void gather_q(unsigned char* ws, int which, int h, u64 T, unsigned* lq, unsigned (&qpk)[12]) {
  const int tid = threadIdx.x, c = tid & 7;
  if (tid < 64) {
    const unsigned goff = WS_QG + (which * 3072 + h * 96 + 2 * (tid < 48 ? tid : 0)) * 8;
    const u64 t1 = T & 0xffffffffull, t2 = T >> 32;
    u64 a = 0, b = 0;
    for (unsigned spins = 0; spins < (1u << 22); ++spins) {
      a = ld_granule(ws, goff);
      b = ld_granule(ws, goff + 8);
      const u64 a2 = ld_granule(ws, goff + 2 * 3072 * 8), b2 = ld_granule(ws, goff + 2 * 3072 * 8 + 8);
      if (__all((a >> 32) == t1 && (b >> 32) == t1 && (a2 >> 32) == t2 && (b2 >> 32) == t2 && (unsigned)a == (unsigned)a2 && (unsigned)b == (unsigned)b2)) break;
      __builtin_amdgcn_s_sleep(2);
    }
    if (tid < 48) { lq[2 * tid] = (unsigned)a; lq[2 * tid + 1] = (unsigned)b; }
  }
  __syncthreads();
#pragma unroll
  for (int i = 0; i < 3; ++i)
#pragma unroll
    for (int k = 0; k < 4; ++k) qpk[i * 4 + k] = lq[i * 32 + c * 4 + k];
}

__device__ __forceinline__ void fma_row(float (&acc)[24], float s, const u32x4 (&w)[3]) {
#pragma unroll
  for (int i = 0; i < 3; ++i)
#pragma unroll
    for (int k = 0; k < 4; ++k) {
      acc[i * 8 + 2 * k] = fmaf(s, bf_lo(w[i][k]), acc[i * 8 + 2 * k]);
      acc[i * 8 + 2 * k + 1] = fmaf(s, bf_hi(w[i][k]), acc[i * 8 + 2 * k + 1]);
    }
}
__device__ __forceinline__ float dot_row(const u32x4 (&w)[3], const unsigned (&q)[12]) {
  float s0 = 0.f, s1 = 0.f, s2 = 0.f;
#pragma unroll
  for (int k = 0; k < 4; ++k) { s0 = dot2(w[0][k], q[k], s0); s1 = dot2(w[1][k], q[4 + k], s1); s2 = dot2(w[2][k], q[8 + k], s2); }
  return (s0 + s1) + s2;
}
__device__ __forceinline__ void a_role(const Params& p, unsigned char* lds, int wg, int h, int split) {
  const u64 T = call_tag(p.ws);
  const int tid = threadIdx.x, wave = __builtin_amdgcn_readfirstlane(tid >> 6), lane = tid & 63, r = lane >> 3, c = lane & 7;
  const int gw = split * 8 + wave;
  const int t0 = (1024 * gw) / 48, t1 = (1024 * (gw + 1)) / 48, nt = t1 - t0;
  const u16* kb = p.Ak + ((size_t)h * SK + r) * DH + c * 8;
  const u16* vb = p.Av + ((size_t)h * SK + r) * DH + c * 8;
  const __amdgpu_buffer_rsrc_t rs = ws_rsrc(p.ws);
  u32x4 kr[4][3], vr[4][3];
  unsigned qpk[12];
  float* red = reinterpret_cast<float*>(lds + LA_RED);
#define A_LOAD(slot, idx) do { const int tt_ = t0 + ((idx) < nt ? (idx) : nt - 1); const size_t o_ = (size_t)tt_ * 8 * DH; \
    _Pragma("unroll") for (int i_ = 0; i_ < 3; ++i_) kr[slot][i_] = ld16(kb + o_ + i_ * 64); \
    _Pragma("unroll") for (int i_ = 0; i_ < 3; ++i_) vr[slot][i_] = ld16(vb + o_ + i_ * 64); } while (0)
  {
    const int which = split >= 3 ? 1 : 0, sl = split - 3 * which;
    const u16* x = which ? p.Bx : p.Ax;
    const u16* W = (which ? p.Bwq : p.Awq) + h * DH + 64 * sl + c * 8;
    float xa[10]; u32x4 wa[10];
#pragma unroll
    for (int t = 0; t < 10; ++t) xa[t] = bf2f(x[wave * 80 + t * 8 + r]);
#pragma unroll
    for (int t = 0; t < 10; ++t) wa[t] = ld16(W + (size_t)(wave * 80 + t * 8 + r) * HD);
#pragma unroll
    for (int d = 0; d < 4; ++d) A_LOAD(d, d);
    float a[8];
#pragma unroll
    for (int k = 0; k < 8; ++k) a[k] = 0.f;
#pragma unroll
    for (int t = 0; t < 10; ++t)
#pragma unroll
      for (int k = 0; k < 4; ++k) { a[2 * k] = fmaf(xa[t], bf_lo(wa[t][k]), a[2 * k]); a[2 * k + 1] = fmaf(xa[t], bf_hi(wa[t][k]), a[2 * k + 1]); }
#pragma unroll
    for (int k = 0; k < 8; ++k) { float v = a[k]; v += dpp<ROR8>(v); v += __shfl_xor(v, 16); v += __shfl_xor(v, 32); a[k] = v; }
    if (r == 0) {
      *reinterpret_cast<f32x4*>(red + wave * 64 + c * 8) = f32x4{a[0], a[1], a[2], a[3]};
      *reinterpret_cast<f32x4*>(red + wave * 64 + c * 8 + 4) = f32x4{a[4], a[5], a[6], a[7]};
    }
    __syncthreads();
    if (tid < 32) {
      float s0 = 0.f, s1 = 0.f;
#pragma unroll
      for (int w8 = 0; w8 < 8; ++w8) { s0 += red[w8 * 64 + 2 * tid]; s1 += red[w8 * 64 + 2 * tid + 1]; }
      const u64 qv = (u64)((unsigned)f2bf(s0) | ((unsigned)f2bf(s1) << 16));
      st_granule(p.ws, WS_QG + (which * 3072 + h * 96 + 32 * sl + tid) * 8, (T << 32) | qv);
      st_granule(p.ws, WS_QG + (2 * 3072 + which * 3072 + h * 96 + 32 * sl + tid) * 8, (T & 0xffffffff00000000ull) | qv);
    }
  }
  {
    u32x4 t[10];
    const u16* wo = p.Awo + (size_t)(h * DH + 32 * split) * DM;
    const u16* wob = p.Bwo + (size_t)(h * DH + 32 * split) * DM;
#pragma unroll
    for (int i = 0; i < 5; ++i) { t[i] = ld16(wo + (tid + 512 * i) * 8); t[5 + i] = ld16(wob + (tid + 512 * i) * 8); }
#pragma unroll
    for (int i = 0; i < 5; ++i) {
      *reinterpret_cast<u32x4*>(lds + LA_WO + (tid + 512 * i) * 16) = t[i];
      *reinterpret_cast<u32x4*>(lds + LA_WOB + (tid + 512 * i) * 16) = t[5 + i];
    }
  }
  gather_q(p.ws, 0, h, T, reinterpret_cast<unsigned*>(lds + LA_Q), qpk);
  STAMP(1);
  float m = -INFINITY, l = 0.f, acc[24];
#pragma unroll
  for (int k = 0; k < 24; ++k) acc[k] = 0.f;
#define A_STEP(slot, idx) do { \
    float s_ = sum8(dot_row(kr[slot], qpk)); \
    s_ = bf16r(bf16r(s_) * SCALE); \
    s_ = ((idx) < nt) ? s_ : -INFINITY; \
    const float mn_ = fmaxf(m, s_); \
    const float al_ = __builtin_amdgcn_exp2f((m - mn_) * LOG2E), pp_ = __builtin_amdgcn_exp2f((s_ - mn_) * LOG2E); \
    l = fmaf(l, al_, pp_); m = mn_; \
    _Pragma("unroll") for (int k_ = 0; k_ < 24; ++k_) acc[k_] *= al_; \
    fma_row(acc, pp_, vr[slot]); } while (0)
  for (int it = 0; it < 5; ++it) {
#pragma unroll
    for (int d = 0; d < 4; ++d) { A_STEP(d, it * 4 + d); A_LOAD(d, it * 4 + d + 4); }
  }
  const u64 zc = flag_load(p.ws, WS_FZ);
#pragma unroll
  for (int d = 0; d < 4; ++d) A_STEP(d, 20 + d);
  flag_wait(p.ws, WS_FZ, T, zc);
#undef A_STEP
#undef A_LOAD
  STAMP(2);
  {
    float M = m; M = fmaxf(M, dpp<ROR8>(M)); M = fmaxf(M, __shfl_xor(M, 16)); M = fmaxf(M, __shfl_xor(M, 32));
    const float f = __builtin_amdgcn_exp2f((m - M) * LOG2E);
    l *= f; l += dpp<ROR8>(l); l += __shfl_xor(l, 16); l += __shfl_xor(l, 32);
#pragma unroll
    for (int k = 0; k < 24; ++k) { float v = acc[k] * f; v += dpp<ROR8>(v); v += __shfl_xor(v, 16); v += __shfl_xor(v, 32); acc[k] = v; }
    if (r == 0) {
#pragma unroll
      for (int i = 0; i < 3; ++i) {
        *reinterpret_cast<f32x4*>(red + wave * 200 + i * 64 + c * 8) = f32x4{acc[i * 8], acc[i * 8 + 1], acc[i * 8 + 2], acc[i * 8 + 3]};
        *reinterpret_cast<f32x4*>(red + wave * 200 + i * 64 + c * 8 + 4) = f32x4{acc[i * 8 + 4], acc[i * 8 + 5], acc[i * 8 + 6], acc[i * 8 + 7]};
      }
      if (c == 0) { red[wave * 200 + 192] = M; red[wave * 200 + 193] = l; }
    }
    __syncthreads();
    if (tid < 194) {
      float MM = -INFINITY;
#pragma unroll
      for (int w8 = 0; w8 < 8; ++w8) MM = fmaxf(MM, red[w8 * 200 + 192]);
      float o = 0.f;
      const int src = tid < 192 ? tid : 193;
#pragma unroll
      for (int w8 = 0; w8 < 8; ++w8) o = fmaf(__builtin_amdgcn_exp2f((red[w8 * 200 + 192] - MM) * LOG2E), red[w8 * 200 + src], o);
      st_granule(p.ws, WS_PG + (wg * 200 + tid) * 8, granule(tid == 192 ? MM : o));
    }
  }
  STAMP(3);
  float* gat = reinterpret_cast<float*>(lds + LA_Q);
  float* sv = gat + 512;
  if (tid < 320) {
    unsigned off;
    if (tid < 256) { const int t_ = tid < 204 ? tid : 0, j = t_ / 34, k = t_ % 34; off = WS_PG + ((h * ASPLIT + j) * 200 + (k < 32 ? 32 * split + k : 160 + k)) * 8; }
    else off = WS_XG + ((h * 2 + ((tid >> 5) & 1)) * DH + 32 * split + (tid & 31)) * 8;
    unsigned long long a = 0;
    for (unsigned spins = 0; spins < (1u << 22); ++spins) {
      a = ld_granule(p.ws, off);
      if (__all((a >> 32) == 1ull)) break;
      __builtin_amdgcn_s_sleep(1);
    }
    gat[tid] = __builtin_bit_cast(float, (unsigned)a);
  }
  __syncthreads();
  STAMP(5);
  if (tid < 32) {
    float MM = -INFINITY;
#pragma unroll
    for (int j = 0; j < ASPLIT; ++j) MM = fmaxf(MM, gat[j * 34 + 32]);
    float o = 0.f, L = 0.f;
#pragma unroll
    for (int j = 0; j < ASPLIT; ++j) { const float wt = __builtin_amdgcn_exp2f((gat[j * 34 + 32] - MM) * LOG2E); o = fmaf(wt, gat[j * 34 + tid], o); L = fmaf(wt, gat[j * 34 + 33], L); }
    sv[tid] = bf16r(o / L);
  } else if (tid < 64) {
    sv[tid] = bf16r(gat[256 + tid - 32] + gat[256 + tid]);
  }
  __syncthreads();
  {
    float* red2 = red;
    const int chunk = tid % 80, rg = tid / 80;
    if (tid < 320) {
      float a[8], b[8];
#pragma unroll
      for (int k = 0; k < 8; ++k) { a[k] = 0.f; b[k] = 0.f; }
#pragma unroll
      for (int i = 0; i < 8; ++i) {
        const float sa = sv[rg * 8 + i], sb = sv[32 + rg * 8 + i];
        const u32x4 wa = *reinterpret_cast<const u32x4*>(lds + LA_WO + (rg * 8 + i) * 1280 + chunk * 16);
        const u32x4 wb = *reinterpret_cast<const u32x4*>(lds + LA_WOB + (rg * 8 + i) * 1280 + chunk * 16);
#pragma unroll
        for (int k = 0; k < 4; ++k) {
          a[2 * k] = fmaf(sa, bf_lo(wa[k]), a[2 * k]); a[2 * k + 1] = fmaf(sa, bf_hi(wa[k]), a[2 * k + 1]);
          b[2 * k] = fmaf(sb, bf_lo(wb[k]), b[2 * k]); b[2 * k + 1] = fmaf(sb, bf_hi(wb[k]), b[2 * k + 1]);
        }
      }
      *reinterpret_cast<f32x4*>(red2 + rg * 640 + chunk * 8) = f32x4{a[0], a[1], a[2], a[3]};
      *reinterpret_cast<f32x4*>(red2 + rg * 640 + chunk * 8 + 4) = f32x4{a[4], a[5], a[6], a[7]};
      *reinterpret_cast<f32x4*>(red2 + 2560 + rg * 640 + chunk * 8) = f32x4{b[0], b[1], b[2], b[3]};
      *reinterpret_cast<f32x4*>(red2 + 2560 + rg * 640 + chunk * 8 + 4) = f32x4{b[4], b[5], b[6], b[7]};
    }
    __syncthreads();
    if (tid < 320) {
      const float* src = red2 + (tid < 160 ? tid * 4 : 2560 + (tid - 160) * 4);
      f32x4 sum = *reinterpret_cast<const f32x4*>(src);
#pragma unroll
      for (int g = 1; g < 4; ++g) sum += *reinterpret_cast<const f32x4*>(src + g * 640);
      const unsigned off = WS_OG + (((tid >> 3) * NA + wg) * 32 + (tid & 7) * 4) * 8;
      st16_dev_u(rs, off, u32x4{f2u(sum[0]), 1u, f2u(sum[1]), 1u});
      st16_dev_u(rs, off + 16, u32x4{f2u(sum[2]), 1u, f2u(sum[3]), 1u});
    }
  }
  STAMP(6);
}

__device__ __forceinline__ void b_role(const Params& p, unsigned char* lds, int bb, int h, int half) {
  const u64 T = call_tag(p.ws);
  const int tid = threadIdx.x, wave = tid >> 6, lane = tid & 63, r = lane >> 3, c = lane & 7, r16 = lane & 15, g = lane >> 4;
  const __amdgpu_buffer_rsrc_t rs = ws_rsrc(p.ws);
  u32x4 cf[24];
  {
#pragma unroll
    for (int i = 0; i < ZPT; ++i) { const unsigned o = (unsigned)(bb * ZPER + tid + 512 * i); if (tid + 512 * i < ZPER) st16_dev_u(rs, WS_XG + o * 16, u32x4{0u, 0u, 0u, 0u}); }
    asm volatile("" ::: "memory");
    u32x4 w[6][3];
#pragma unroll
    for (int t = 0; t < 6; ++t) {
      const u16* q_ = p.Bwuk + (size_t)(384 * half + 48 * wave + 8 * t + r) * HD + h * DH + c * 8;
#pragma unroll
      for (int i = 0; i < 3; ++i) w[t][i] = ld16(q_ + i * 64);
    }
    asm volatile("s_waitcnt vmcnt(18)" ::: "memory");
    __syncthreads();
    if (tid == 64) set_flag(p.ws, WS_FZ, bb, T);
    unsigned qpk[12];
    gather_q(p.ws, 1, h, T, reinterpret_cast<unsigned*>(lds + L_Q), qpk);
    u16* sh = reinterpret_cast<u16*>(lds + L_RED);
#pragma unroll
    for (int t = 0; t < 6; ++t) {
      const float s = sum8(dot_row(w[t], qpk));
      const u16 hi = f2bf(s); const u16 lo = f2bf(s - bf2f(hi));
      if (c == 0) { sh[48 * wave + 8 * t + r] = hi; sh[384 + 48 * wave + 8 * t + r] = lo; }
    }
    __syncthreads();
    if (tid < 96) {
      const int part = tid / 48, ch = tid % 48;
      const u32x4 v = *reinterpret_cast<const u32x4*>(sh + part * 384 + ch * 8);
      st16_dev_u(rs, WS_QABS + ((part * H + h) * DC + 384 * half + ch * 8) * 2, v);
    }
    publish(p.ws, WS_F1, bb, T);
  }
  STAMP(1);
  {
    u32x4 raw[24];
    const u16* cp = p.Bc + (size_t)(128 * bb + 16 * wave + r) * DC + 8 * c;
#pragma unroll
    for (int hf = 0; hf < 2; ++hf)
#pragma unroll
      for (int rg = 0; rg < 2; ++rg)
#pragma unroll
        for (int i = 0; i < 6; ++i) raw[hf * 12 + rg * 6 + i] = ld16(cp + (size_t)(8 * rg) * DC + 384 * hf + 64 * i);
#pragma unroll
    for (int hf = 0; hf < 2; ++hf) {
#pragma unroll
      for (int rg = 0; rg < 2; ++rg)
#pragma unroll
        for (int i = 0; i < 6; ++i)
          *reinterpret_cast<u32x4*>(lds + L_BIG + (16 * wave + 8 * rg + r) * CROW + (64 * i + 8 * c) * 2) = raw[hf * 12 + rg * 6 + i];
      asm volatile("s_waitcnt lgkmcnt(0)" ::: "memory");
#pragma unroll
      for (int s = 0; s < 12; ++s) cf[12 * hf + s] = *reinterpret_cast<const u32x4*>(lds + L_BIG + (16 * wave + r16) * CROW + (32 * s + 8 * g) * 2);
      asm volatile("s_waitcnt lgkmcnt(0)" ::: "memory");
    }
  }
  wait_flags(p.ws, WS_F1, T);
  STAMP(2);
#pragma unroll
  for (int i = 0; i < 12; ++i) {
    const int q = tid + 512 * i;
    const int row = q / 96, ch = q % 96;
    const u32x4 v = ld16_dev_u(rs, WS_QABS + q * 16);
    *reinterpret_cast<u32x4*>(lds + L_BIG + row * QROW + ch * 16) = v;
  }
  u32x4 wv[6][3];
#pragma unroll
  for (int t = 0; t < 6; ++t) {
    const u16* q_ = p.Bwuv + (size_t)(384 * half + 48 * wave + 8 * t + r) * HD + h * DH + c * 8;
#pragma unroll
    for (int i = 0; i < 3; ++i) wv[t][i] = ld16(q_ + i * 64);
  }
  __syncthreads();
  f32x4 sc[2] = {f32x4{0.f, 0.f, 0.f, 0.f}, f32x4{0.f, 0.f, 0.f, 0.f}};
#pragma unroll
  for (int s = 0; s < 24; ++s) {
#pragma unroll
    for (int ht = 0; ht < 2; ++ht) {
      const s16x8 bh = *reinterpret_cast<const s16x8*>(lds + L_BIG + (16 * ht + r16) * QROW + (32 * s + 8 * g) * 2);
      const s16x8 bl = *reinterpret_cast<const s16x8*>(lds + L_BIG + (32 + 16 * ht + r16) * QROW + (32 * s + 8 * g) * 2);
      sc[ht] = __builtin_amdgcn_mfma_f32_16x16x32_bf16(__builtin_bit_cast(s16x8, cf[s]), bh, sc[ht], 0, 0, 0);
      sc[ht] = __builtin_amdgcn_mfma_f32_16x16x32_bf16(__builtin_bit_cast(s16x8, cf[s]), bl, sc[ht], 0, 0, 0);
    }
  }
  float* wmax = reinterpret_cast<float*>(lds + L_ST);
  float* wsum = wmax + 256;
#pragma unroll
  for (int ht = 0; ht < 2; ++ht) {
#pragma unroll
    for (int k = 0; k < 4; ++k) sc[ht][k] = bf16r(bf16r(sc[ht][k]) * SCALE);
    float mx = fmaxf(fmaxf(sc[ht][0], sc[ht][1]), fmaxf(sc[ht][2], sc[ht][3]));
    mx = fmaxf(mx, __shfl_xor(mx, 16)); mx = fmaxf(mx, __shfl_xor(mx, 32));
    if (g == 0) wmax[wave * 32 + 16 * ht + r16] = mx;
  }
  __syncthreads();
#pragma unroll
  for (int ht = 0; ht < 2; ++ht) {
    float M = -INFINITY;
#pragma unroll
    for (int w8 = 0; w8 < 8; ++w8) M = fmaxf(M, wmax[w8 * 32 + 16 * ht + r16]);
    float e[4], sum = 0.f;
#pragma unroll
    for (int k = 0; k < 4; ++k) { e[k] = __builtin_amdgcn_exp2f((sc[ht][k] - M) * LOG2E); sum += e[k]; }
    sum += __shfl_xor(sum, 16); sum += __shfl_xor(sum, 32);
    if (g == 0) wsum[wave * 32 + 16 * ht + r16] = sum;
    u32x2 pk = {(unsigned)f2bf(e[0]) | ((unsigned)f2bf(e[1]) << 16), (unsigned)f2bf(e[2]) | ((unsigned)f2bf(e[3]) << 16)};
    *reinterpret_cast<u32x2*>(lds + L_P + (16 * ht + r16) * PROW + (16 * wave + 4 * g) * 2) = pk;
  }
#pragma unroll
  for (int s = 0; s < 12; ++s) *reinterpret_cast<u32x4*>(lds + L_BIG + (16 * wave + r16) * CROW + (32 * s + 8 * g) * 2) = cf[s];
  __syncthreads();
  if (tid < 32) {
    float M = -INFINITY, L = 0.f;
#pragma unroll
    for (int w8 = 0; w8 < 8; ++w8) { M = fmaxf(M, wmax[w8 * 32 + tid]); L += wsum[w8 * 32 + tid]; }
    st8_dev(p.ws, WS_BSTAT + (bb * H + tid) * 8, M, L);
  }
  {
    s16x8 pf[2][4];
#pragma unroll
    for (int ht = 0; ht < 2; ++ht)
#pragma unroll
      for (int ks = 0; ks < 4; ++ks) pf[ht][ks] = *reinterpret_cast<const s16x8*>(lds + L_P + (16 * ht + r16) * PROW + (32 * ks + 8 * g) * 2);
    const int q4 = r16 >> 2, p4 = r16 & 3;
#pragma unroll
    for (int ch = 0; ch < 2; ++ch) {
      if (ch == 1) {
        __syncthreads();
#pragma unroll
        for (int s = 0; s < 12; ++s) *reinterpret_cast<u32x4*>(lds + L_BIG + (16 * wave + r16) * CROW + (32 * s + 8 * g) * 2) = cf[12 + s];
        __syncthreads();
      }
#pragma unroll
      for (int ct = 0; ct < 3; ++ct) {
        f32x4 d[2] = {f32x4{0.f, 0.f, 0.f, 0.f}, f32x4{0.f, 0.f, 0.f, 0.f}};
#pragma unroll
        for (int ks = 0; ks < 4; ++ks) {
          const unsigned char* a0 = lds + L_BIG + (32 * ks + 8 * g + q4) * CROW + (48 * wave + 16 * ct + 4 * p4) * 2;
          typedef short v4i16_t __attribute__((ext_vector_type(4)));
          const v4i16_t t0 = __builtin_amdgcn_ds_read_tr16_b64_v4i16((LAS v4i16_t*)(a0));
          const v4i16_t t1 = __builtin_amdgcn_ds_read_tr16_b64_v4i16((LAS v4i16_t*)(a0 + 4 * CROW));
          const s16x8 af = {t0[0], t0[1], t0[2], t0[3], t1[0], t1[1], t1[2], t1[3]};
#pragma unroll
          for (int ht = 0; ht < 2; ++ht) d[ht] = __builtin_amdgcn_mfma_f32_16x16x32_bf16(af, pf[ht][ks], d[ht], 0, 0, 0);
        }
#pragma unroll
        for (int ht = 0; ht < 2; ++ht)
          st16_dev(rs, WS_BCTX + (unsigned)(((bb * H + 16 * ht + r16) * DC + 384 * ch + 48 * wave + 16 * ct + 4 * g) * 4), d[ht]);
      }
    }
  }
  publish(p.ws, WS_F2, bb, T);
  STAMP(3);
  flag_wait(p.ws, WS_FZ, T, flag_load(p.ws, WS_FZ));
  wait_flags(p.ws, WS_F2, T);
  STAMP(4);
  {
    float* coef = reinterpret_cast<float*>(lds + L_ST);
    float* part = reinterpret_cast<float*>(lds + L_BIG);
    float* red = reinterpret_cast<float*>(lds + L_RED);
    const int jg = tid / 96, q = tid % 96;
    f32x4 v[16];
    if (tid < 384) {
#pragma unroll
      for (int j = 0; j < 16; ++j) v[j] = ld16_dev(rs, WS_BCTX + (unsigned)((((16 * jg + j) * H + h) * DC + 384 * half + 4 * q) * 4));
    }
    if (tid >= 448) {
      const int j = tid - 448;
      const f32x2 ml = ld8_dev(p.ws, WS_BSTAT + (j * H + h) * 8);
      float M = ml[0];
#pragma unroll
      for (int o = 1; o < 64; o <<= 1) M = fmaxf(M, __shfl_xor(M, o));
      const float wt = __builtin_amdgcn_exp2f((ml[0] - M) * LOG2E);
      float L = wt * ml[1];
#pragma unroll
      for (int o = 1; o < 64; o <<= 1) L += __shfl_xor(L, o);
      coef[j] = wt / L;
    }
    __syncthreads();
    if (tid < 384) {
      f32x4 a = {0.f, 0.f, 0.f, 0.f};
#pragma unroll
      for (int j = 0; j < 16; ++j) a += coef[16 * jg + j] * v[j];
      *reinterpret_cast<f32x4*>(part + jg * 384 + 4 * q) = a;
    }
    __syncthreads();
    float acc[24];
#pragma unroll
    for (int k = 0; k < 24; ++k) acc[k] = 0.f;
#pragma unroll
    for (int t = 0; t < 6; ++t) { const int row = 48 * wave + 8 * t + r; fma_row(acc, (part[row] + part[384 + row]) + (part[768 + row] + part[1152 + row]), wv[t]); }
#pragma unroll
    for (int k = 0; k < 24; ++k) { float x = acc[k]; x += dpp<ROR8>(x); x += __shfl_xor(x, 16); x += __shfl_xor(x, 32); acc[k] = x; }
    if (r == 0) {
#pragma unroll
      for (int i = 0; i < 3; ++i) {
        *reinterpret_cast<f32x4*>(red + wave * 192 + i * 64 + c * 8) = f32x4{acc[i * 8], acc[i * 8 + 1], acc[i * 8 + 2], acc[i * 8 + 3]};
        *reinterpret_cast<f32x4*>(red + wave * 192 + i * 64 + c * 8 + 4) = f32x4{acc[i * 8 + 4], acc[i * 8 + 5], acc[i * 8 + 6], acc[i * 8 + 7]};
      }
    }
    __syncthreads();
    if (tid < 192) {
      float s = 0.f;
#pragma unroll
      for (int w8 = 0; w8 < 8; ++w8) s += red[w8 * 192 + tid];
      st_granule(p.ws, WS_XG + ((h * 2 + half) * DH + tid) * 8, granule(s));
    }
  }
  STAMP(5);
  if (bb < 40) {
    float* buf = reinterpret_cast<float*>(lds + L_BIG);
    float* red = reinterpret_cast<float*>(lds + L_RED);
    __syncthreads();
    {
      const int j = tid >> 4, q = tid & 15;
      u32x4 v[6];
      for (unsigned spins = 0; spins < (1u << 22); ++spins) {
        bool ok = true;
#pragma unroll
        for (int i = 0; i < 6; ++i) { v[i] = ld16_dev_u(rs, WS_OG + ((bb * NA + j + 32 * i) * 32 + 2 * q) * 8); ok = ok && v[i][1] == 1u && v[i][3] == 1u; }
        if (__all(ok)) break;
        __builtin_amdgcn_s_sleep(4);
      }
#pragma unroll
      for (int i = 0; i < 6; ++i) { buf[(j + 32 * i) * 32 + 2 * q] = u2f(v[i][0]); buf[(j + 32 * i) * 32 + 2 * q + 1] = u2f(v[i][2]); }
    }
    STAMP(7);
    __syncthreads();
    if (tid < 256) {
      const int part = tid >> 5, col = tid & 31; float s = 0.f;
#pragma unroll
      for (int j = 0; j < 24; ++j) s += buf[(part * 24 + j) * 32 + col];
      red[tid] = s;
    }
    __syncthreads();
    if (tid < 32) {
      float s = 0.f;
#pragma unroll
      for (int j = 0; j < 8; ++j) s += red[j * 32 + tid];
      p.out[32 * bb + tid] = f2bf(s);
    }
    if (bb == 0 && tid == 0) {
      GAS u64* e = (GAS u64*)(p.ws + WS_EPOCH);
      __hip_atomic_store(e, __hip_atomic_load(e, __ATOMIC_RELAXED, __HIP_MEMORY_SCOPE_AGENT) + 1ull, __ATOMIC_RELAXED, __HIP_MEMORY_SCOPE_AGENT);
    }
  }
  STAMP(8);
}

__global__ void __launch_bounds__(NTHR, 2) fused_decode_kernel(Params p) {
  __shared__ __attribute__((aligned(16))) unsigned char lds[LDS_BYTES];
  const int b = blockIdx.x;
  STAMP(0);
  if (b < NA) {
    const int x = b & 7, idx = b >> 3;
    const int h = x * 4 + idx / ASPLIT, split = idx % ASPLIT;
    a_role(p, lds, h * ASPLIT + split, h, split);
  } else {
    const int bb = b - NA;
    b_role(p, lds, bb, (bb & 7) * 4 + (bb >> 4), (bb >> 3) & 1);
  }
}

extern "C" void kernel_launch(void* const* d_in, const int* in_sizes, int n_in,
                              void* d_out, int out_size, void* d_ws, size_t ws_size,
                              hipStream_t stream) {
  Params p;
  p.Ax = (const u16*)d_in[0]; p.Ak = (const u16*)d_in[1]; p.Av = (const u16*)d_in[2]; p.Awq = (const u16*)d_in[3]; p.Awo = (const u16*)d_in[4];
  p.Bx = (const u16*)d_in[5]; p.Bc = (const u16*)d_in[6]; p.Bwq = (const u16*)d_in[7]; p.Bwuk = (const u16*)d_in[8]; p.Bwuv = (const u16*)d_in[9]; p.Bwo = (const u16*)d_in[10];
  p.out = (u16*)d_out; p.ws = (unsigned char*)d_ws;
  fused_decode_kernel<<<NA + NB, NTHR, 0, stream>>>(p);
}
```

```cpp
#include <hip/hip_runtime.h>
#include <hip/hip_bf16.h>
#include <stdint.h>

#define GAS __attribute__((address_space(1)))
#define LAS __attribute__((address_space(3)))
typedef unsigned u32x4 __attribute__((ext_vector_type(4)));
typedef unsigned u32x2 __attribute__((ext_vector_type(2)));
typedef float f32x4 __attribute__((ext_vector_type(4)));
typedef float f32x2 __attribute__((ext_vector_type(2)));
typedef short s16x8 __attribute__((ext_vector_type(8)));
typedef short s16x4 __attribute__((ext_vector_type(4)));
typedef __bf16 bf16x2_t __attribute__((ext_vector_type(2)));
typedef unsigned short u16;

constexpr int H = 32, SK = 8192, DH = 192, DM = 640, DC = 768, HD = H * DH;
constexpr int NA = 192, NB = 64, NTHR = 512, ASPLIT = 6;
constexpr float SCALE = 0.07216878364870322f;
constexpr float LOG2E = 1.4426950408889634f;

constexpr unsigned WS_EPOCH = 0;
constexpr unsigned WS_FZ = 1024, WS_F1 = 2048, WS_F2 = 3072;
constexpr unsigned WS_QG = 4096;
constexpr unsigned WS_XG = 131072;
constexpr unsigned WS_PG = WS_XG + H * 2 * DH * 8;
constexpr unsigned WS_OG = WS_PG + NA * 200 * 8;
constexpr unsigned WS_OGB = WS_OG + 20 * NA * 32 * 8;
constexpr unsigned WS_QABS = WS_OGB + 20 * NB * 32 * 8;
constexpr unsigned WS_BSTAT = WS_QABS + 2 * H * DC * 2;
constexpr unsigned WS_BCTX = WS_BSTAT + NB * H * 2 * 4;
constexpr unsigned WS_END = WS_BCTX + NB * H * DC * 4;
static_assert(WS_QG + 4 * 3072 * 8 <= WS_XG, "ws");
constexpr int ZPER = (int)((WS_QABS - WS_XG) / 16 / NB), ZPT = (ZPER + 511) / 512;
static_assert((WS_QABS - WS_XG) % (16 * NB) == 0, "ws");

constexpr int QROW = 1552;
constexpr int CROW = 784;
constexpr int PROW = 272;
constexpr int L_BIG = 0;
constexpr int L_P = 100352;
constexpr int L_ST = L_P + 32 * PROW;
constexpr int L_RED = L_ST + 2048;
constexpr int L_Q = L_RED + 16384;
constexpr int LB_WO = 0, LB_PART = 122880, LB_COEF = LB_PART + 6144, LB_RED = LB_COEF + 512, LB_CS = LB_RED + 6144, LB_RED2 = LB_CS + 1536;
constexpr int LDS_BYTES = LB_RED2 + 15360;
static_assert(L_Q + 1024 <= LDS_BYTES, "lds");
constexpr int LA_WO = 0;
constexpr int LA_RED = 40960;
constexpr int LA_Q = LA_RED + 24576;
static_assert(LA_Q + 4096 <= LDS_BYTES, "lds");

#define STAMP(i) do { } while (0)
struct Params {
  const u16* Ax; const u16* Ak; const u16* Av; const u16* Awq; const u16* Awo;
  const u16* Bx; const u16* Bc; const u16* Bwq; const u16* Bwuk; const u16* Bwuv; const u16* Bwo;
  u16* out; unsigned char* ws;
};

__device__ __forceinline__ unsigned f2u(float x) { return __builtin_bit_cast(unsigned, x); }
__device__ __forceinline__ float u2f(unsigned x) { return __builtin_bit_cast(float, x); }
__device__ __forceinline__ float bf_lo(unsigned u) { return __builtin_bit_cast(float, u << 16); }
__device__ __forceinline__ float bf_hi(unsigned u) { return __builtin_bit_cast(float, u & 0xffff0000u); }
__device__ __forceinline__ float bf2f(u16 u) { return __builtin_bit_cast(float, (unsigned)u << 16); }
__device__ __forceinline__ u16 f2bf(float x) { __bf16 b = (__bf16)x; return __builtin_bit_cast(u16, b); }
__device__ __forceinline__ float bf16r(float x) { return bf2f(f2bf(x)); }
__device__ __forceinline__ float dot2(unsigned a, unsigned b, float acc) {
  return __builtin_amdgcn_fdot2_f32_bf16(__builtin_bit_cast(bf16x2_t, a), __builtin_bit_cast(bf16x2_t, b), acc, false);
}
template <int CTRL> __device__ __forceinline__ float dpp(float x) {
  return __builtin_bit_cast(float, __builtin_amdgcn_mov_dpp(__builtin_bit_cast(int, x), CTRL, 0xf, 0xf, true));
}
constexpr int XOR1 = 0xB1, XOR2 = 0x4E, HMIRROR = 0x141, ROR8 = 0x128, ROR4 = 0x124;
__device__ __forceinline__ float sum8(float s) {
  s += dpp<XOR1>(s); s += dpp<XOR2>(s); s += dpp<HMIRROR>(s); return s;
}
__device__ __forceinline__ u32x4 ld16(const u16* p) { return __builtin_nontemporal_load(reinterpret_cast<const u32x4*>(p)); }

__device__ __forceinline__ __amdgpu_buffer_rsrc_t ws_rsrc(unsigned char* ws) {
  return __builtin_amdgcn_make_buffer_rsrc(ws, 0, (int)WS_END, 0x00020000);
}
__device__ __forceinline__ void st16_dev(__amdgpu_buffer_rsrc_t r, unsigned off, f32x4 v) {
  __builtin_amdgcn_raw_buffer_store_b128(__builtin_bit_cast(u32x4, v), r, off, 0, 16);
}
__device__ __forceinline__ void st16_dev_u(__amdgpu_buffer_rsrc_t r, unsigned off, u32x4 v) {
  __builtin_amdgcn_raw_buffer_store_b128(v, r, off, 0, 16);
}
__device__ __forceinline__ f32x4 ld16_dev(__amdgpu_buffer_rsrc_t r, unsigned off) {
  return __builtin_bit_cast(f32x4, __builtin_amdgcn_raw_buffer_load_b128(r, off, 0, 16));
}
__device__ __forceinline__ u32x4 ld16_dev_u(__amdgpu_buffer_rsrc_t r, unsigned off) {
  return __builtin_amdgcn_raw_buffer_load_b128(r, off, 0, 16);
}
__device__ __forceinline__ void st4_dev(unsigned char* ws, unsigned off, float v) {
  __hip_atomic_store((GAS float*)(ws + off), v, __ATOMIC_RELAXED, __HIP_MEMORY_SCOPE_AGENT);
}
__device__ __forceinline__ void st4_dev_u(unsigned char* ws, unsigned off, unsigned v) {
  __hip_atomic_store((GAS unsigned*)(ws + off), v, __ATOMIC_RELAXED, __HIP_MEMORY_SCOPE_AGENT);
}
__device__ __forceinline__ float ld4_dev(unsigned char* ws, unsigned off) {
  return __hip_atomic_load((GAS float*)(ws + off), __ATOMIC_RELAXED, __HIP_MEMORY_SCOPE_AGENT);
}
__device__ __forceinline__ void st8_dev(unsigned char* ws, unsigned off, float a, float b) {
  f32x2 v = {a, b};
  __hip_atomic_store((GAS unsigned long long*)(ws + off), __builtin_bit_cast(unsigned long long, v), __ATOMIC_RELAXED, __HIP_MEMORY_SCOPE_AGENT);
}
__device__ __forceinline__ f32x2 ld8_dev(unsigned char* ws, unsigned off) {
  unsigned long long v = __hip_atomic_load((GAS unsigned long long*)(ws + off), __ATOMIC_RELAXED, __HIP_MEMORY_SCOPE_AGENT);
  return __builtin_bit_cast(f32x2, v);
}
typedef unsigned long long u64;
__device__ __forceinline__ u64 call_tag(unsigned char* ws) {
  u64 z = __hip_atomic_load((const GAS u64*)(ws + WS_EPOCH), __ATOMIC_RELAXED, __HIP_MEMORY_SCOPE_AGENT) + 1ull;
  z *= 0x9E3779B97F4A7C15ull;
  z = (z ^ (z >> 30)) * 0xBF58476D1CE4E5B9ull; z = (z ^ (z >> 27)) * 0x94D049BB133111EBull;
  return z ^ (z >> 31);
}
__device__ __forceinline__ void set_flag(unsigned char* ws, unsigned base, int i, u64 T) {
  __hip_atomic_store((GAS u64*)(ws + base + i * 8), T, __ATOMIC_RELAXED, __HIP_MEMORY_SCOPE_AGENT);
}
__device__ __forceinline__ u64 flag_load(unsigned char* ws, unsigned base) {
  return __hip_atomic_load((const GAS u64*)(ws + base + (threadIdx.x & 63) * 8), __ATOMIC_RELAXED, __HIP_MEMORY_SCOPE_AGENT);
}
__device__ __forceinline__ void flag_wait(unsigned char* ws, unsigned base, u64 T, u64 z) {
  for (unsigned spins = 0; spins < (1u << 22); ++spins) {
    if (__all(z == T)) break;
    __builtin_amdgcn_s_sleep(2);
    z = flag_load(ws, base);
  }
}
__device__ __forceinline__ void publish(unsigned char* ws, unsigned base, int i, u64 T) {
  asm volatile("s_waitcnt vmcnt(0)" ::: "memory");
  __syncthreads();
  if (threadIdx.x == 0) set_flag(ws, base, i, T);
}
__device__ __forceinline__ void wait_flags(unsigned char* ws, unsigned base, u64 T) {
  if (threadIdx.x < 64) flag_wait(ws, base, T, flag_load(ws, base));
  __syncthreads();
}

__device__ __forceinline__ unsigned long long granule(float v) { return (1ull << 32) | (unsigned long long)__builtin_bit_cast(unsigned, v); }
__device__ __forceinline__ void st_granule(unsigned char* ws, unsigned off, unsigned long long gv) {
  __hip_atomic_store((GAS unsigned long long*)(ws + off), gv, __ATOMIC_RELAXED, __HIP_MEMORY_SCOPE_AGENT);
}
__device__ __forceinline__ unsigned long long ld_granule(unsigned char* ws, unsigned off) {
  return __hip_atomic_load((const GAS unsigned long long*)(ws + off), __ATOMIC_RELAXED, __HIP_MEMORY_SCOPE_AGENT);
}
__device__ __forceinline__ void gather_q(unsigned char* ws, int which, int h, u64 T, unsigned* lq, unsigned (&qpk)[12]) {
  const int tid = threadIdx.x, c = tid & 7;
  if (tid < 64) {
    const unsigned goff = WS_QG + (which * 3072 + h * 96 + 2 * (tid < 48 ? tid : 0)) * 8;
    const u64 t1 = T & 0xffffffffull, t2 = T >> 32;
    u64 a = 0, b = 0;
    for (unsigned spins = 0; spins < (1u << 22); ++spins) {
      a = ld_granule(ws, goff);
      b = ld_granule(ws, goff + 8);
      const u64 a2 = ld_granule(ws, goff + 2 * 3072 * 8), b2 = ld_granule(ws, goff + 2 * 3072 * 8 + 8);
      if (__all((a >> 32) == t1 && (b >> 32) == t1 && (a2 >> 32) == t2 && (b2 >> 32) == t2 && (unsigned)a == (unsigned)a2 && (unsigned)b == (unsigned)b2)) break;
      __builtin_amdgcn_s_sleep(2);
    }
    if (tid < 48) { lq[2 * tid] = (unsigned)a; lq[2 * tid + 1] = (unsigned)b; }
  }
  __syncthreads();
#pragma unroll
  for (int i = 0; i < 3; ++i)
#pragma unroll
    for (int k = 0; k < 4; ++k) qpk[i * 4 + k] = lq[i * 32 + c * 4 + k];
}

__device__ __forceinline__ void fma_row(float (&acc)[24], float s, const u32x4 (&w)[3]) {
#pragma unroll
  for (int i = 0; i < 3; ++i)
#pragma unroll
    for (int k = 0; k < 4; ++k) {
      acc[i * 8 + 2 * k] = fmaf(s, bf_lo(w[i][k]), acc[i * 8 + 2 * k]);
      acc[i * 8 + 2 * k + 1] = fmaf(s, bf_hi(w[i][k]), acc[i * 8 + 2 * k + 1]);
    }
}
__device__ __forceinline__ float dot_row(const u32x4 (&w)[3], const unsigned (&q)[12]) {
  float s0 = 0.f, s1 = 0.f, s2 = 0.f;
#pragma unroll
  for (int k = 0; k < 4; ++k) { s0 = dot2(w[0][k], q[k], s0); s1 = dot2(w[1][k], q[4 + k], s1); s2 = dot2(w[2][k], q[8 + k], s2); }
  return (s0 + s1) + s2;
}
__device__ __forceinline__ void a_role(const Params& p, unsigned char* lds, int wg, int h, int split) {
  const u64 T = call_tag(p.ws);
  const int tid = threadIdx.x, wave = __builtin_amdgcn_readfirstlane(tid >> 6), lane = tid & 63, r = lane >> 3, c = lane & 7;
  const int gw = split * 8 + wave;
  const int t0 = (1024 * gw) / 48, t1 = (1024 * (gw + 1)) / 48, nt = t1 - t0;
  const u16* kb = p.Ak + ((size_t)h * SK + r) * DH + c * 8;
  const u16* vb = p.Av + ((size_t)h * SK + r) * DH + c * 8;
  const __amdgpu_buffer_rsrc_t rs = ws_rsrc(p.ws);
  u32x4 kr[4][3], vr[4][3];
  unsigned qpk[12];
  float* red = reinterpret_cast<float*>(lds + LA_RED);
#define A_LOAD(slot, idx) do { const int tt_ = t0 + ((idx) < nt ? (idx) : nt - 1); const size_t o_ = (size_t)tt_ * 8 * DH; \
    _Pragma("unroll") for (int i_ = 0; i_ < 3; ++i_) kr[slot][i_] = ld16(kb + o_ + i_ * 64); \
    _Pragma("unroll") for (int i_ = 0; i_ < 3; ++i_) vr[slot][i_] = ld16(vb + o_ + i_ * 64); } while (0)
  {
    const int which = split >= 3 ? 1 : 0, sl = split - 3 * which;
    const u16* x = which ? p.Bx : p.Ax;
    const u16* W = (which ? p.Bwq : p.Awq) + h * DH + 64 * sl + c * 8;
    float xa[10]; u32x4 wa[10];
#pragma unroll
    for (int t = 0; t < 10; ++t) xa[t] = bf2f(x[wave * 80 + t * 8 + r]);
#pragma unroll
    for (int t = 0; t < 10; ++t) wa[t] = ld16(W + (size_t)(wave * 80 + t * 8 + r) * HD);
#pragma unroll
    for (int d = 0; d < 4; ++d) A_LOAD(d, d);
    float a[8];
#pragma unroll
    for (int k = 0; k < 8; ++k) a[k] = 0.f;
#pragma unroll
    for (int t = 0; t < 10; ++t)
#pragma unroll
      for (int k = 0; k < 4; ++k) { a[2 * k] = fmaf(xa[t], bf_lo(wa[t][k]), a[2 * k]); a[2 * k + 1] = fmaf(xa[t], bf_hi(wa[t][k]), a[2 * k + 1]); }
#pragma unroll
    for (int k = 0; k < 8; ++k) { float v = a[k]; v += dpp<ROR8>(v); v += __shfl_xor(v, 16); v += __shfl_xor(v, 32); a[k] = v; }
    if (r == 0) {
      *reinterpret_cast<f32x4*>(red + wave * 64 + c * 8) = f32x4{a[0], a[1], a[2], a[3]};
      *reinterpret_cast<f32x4*>(red + wave * 64 + c * 8 + 4) = f32x4{a[4], a[5], a[6], a[7]};
    }
    asm volatile("" :: "v"(T));
    __syncthreads();
    if (tid < 32) {
      float s0 = 0.f, s1 = 0.f;
#pragma unroll
      for (int w8 = 0; w8 < 8; ++w8) { s0 += red[w8 * 64 + 2 * tid]; s1 += red[w8 * 64 + 2 * tid + 1]; }
      const u64 qv = (u64)((unsigned)f2bf(s0) | ((unsigned)f2bf(s1) << 16));
      st_granule(p.ws, WS_QG + (which * 3072 + h * 96 + 32 * sl + tid) * 8, (T << 32) | qv);
      st_granule(p.ws, WS_QG + (2 * 3072 + which * 3072 + h * 96 + 32 * sl + tid) * 8, (T & 0xffffffff00000000ull) | qv);
    }
  }
  gather_q(p.ws, 0, h, T, reinterpret_cast<unsigned*>(lds + LA_Q), qpk);
  STAMP(1);
  float m = -INFINITY, l = 0.f, acc[24];
#pragma unroll
  for (int k = 0; k < 24; ++k) acc[k] = 0.f;
#define A_STEP(slot, idx) do { \
    float s_ = sum8(dot_row(kr[slot], qpk)); \
    s_ = bf16r(bf16r(s_) * SCALE); \
    s_ = ((idx) < nt) ? s_ : -INFINITY; \
    const float mn_ = fmaxf(m, s_); \
    const float al_ = __builtin_amdgcn_exp2f((m - mn_) * LOG2E), pp_ = __builtin_amdgcn_exp2f((s_ - mn_) * LOG2E); \
    l = fmaf(l, al_, pp_); m = mn_; \
    _Pragma("unroll") for (int k_ = 0; k_ < 24; ++k_) acc[k_] *= al_; \
    fma_row(acc, pp_, vr[slot]); } while (0)
  for (int it = 0; it < 5; ++it) {
#pragma unroll
    for (int d = 0; d < 4; ++d) { A_STEP(d, it * 4 + d); A_LOAD(d, it * 4 + d + 4); }
  }
  const u64 zc = flag_load(p.ws, WS_FZ);
  u32x4 wot[5];
#pragma unroll
  for (int i = 0; i < 5; ++i) wot[i] = ld16(p.Awo + (size_t)(h * DH + 32 * split) * DM + (tid + 512 * i) * 8);
#pragma unroll
  for (int d = 0; d < 4; ++d) A_STEP(d, 20 + d);
  flag_wait(p.ws, WS_FZ, T, zc);
#pragma unroll
  for (int i = 0; i < 5; ++i) *reinterpret_cast<u32x4*>(lds + LA_WO + (tid + 512 * i) * 16) = wot[i];
#undef A_STEP
#undef A_LOAD
  STAMP(2);
  {
    float M = m; M = fmaxf(M, dpp<ROR8>(M)); M = fmaxf(M, __shfl_xor(M, 16)); M = fmaxf(M, __shfl_xor(M, 32));
    const float f = __builtin_amdgcn_exp2f((m - M) * LOG2E);
    l *= f; l += dpp<ROR8>(l); l += __shfl_xor(l, 16); l += __shfl_xor(l, 32);
#pragma unroll
    for (int k = 0; k < 24; ++k) { float v = acc[k] * f; v += dpp<ROR8>(v); v += __shfl_xor(v, 16); v += __shfl_xor(v, 32); acc[k] = v; }
    if (r == 0) {
#pragma unroll
      for (int i = 0; i < 3; ++i) {
        *reinterpret_cast<f32x4*>(red + wave * 200 + i * 64 + c * 8) = f32x4{acc[i * 8], acc[i * 8 + 1], acc[i * 8 + 2], acc[i * 8 + 3]};
        *reinterpret_cast<f32x4*>(red + wave * 200 + i * 64 + c * 8 + 4) = f32x4{acc[i * 8 + 4], acc[i * 8 + 5], acc[i * 8 + 6], acc[i * 8 + 7]};
      }
      if (c == 0) { red[wave * 200 + 192] = M; red[wave * 200 + 193] = l; }
    }
    __syncthreads();
    if (tid < 194) {
      float MM = -INFINITY;
#pragma unroll
      for (int w8 = 0; w8 < 8; ++w8) MM = fmaxf(MM, red[w8 * 200 + 192]);
      float o = 0.f;
      const int src = tid < 192 ? tid : 193;
#pragma unroll
      for (int w8 = 0; w8 < 8; ++w8) o = fmaf(__builtin_amdgcn_exp2f((red[w8 * 200 + 192] - MM) * LOG2E), red[w8 * 200 + src], o);
      st_granule(p.ws, WS_PG + (wg * 200 + tid) * 8, granule(tid == 192 ? MM : o));
    }
  }
  STAMP(3);
  float* gat = reinterpret_cast<float*>(lds + LA_Q);
  float* sv = gat + 512;
  if (tid < 256) {
    const int t_ = tid < 204 ? tid : 0, j = t_ / 34, k = t_ % 34;
    const unsigned off = WS_PG + ((h * ASPLIT + j) * 200 + (k < 32 ? 32 * split + k : 160 + k)) * 8;
    unsigned long long a = 0;
    for (unsigned spins = 0; spins < (1u << 22); ++spins) {
      a = ld_granule(p.ws, off);
      if (__all((a >> 32) == 1ull)) break;
      __builtin_amdgcn_s_sleep(1);
    }
    gat[tid] = __builtin_bit_cast(float, (unsigned)a);
  }
  __syncthreads();
  STAMP(5);
  if (tid < 32) {
    float MM = -INFINITY;
#pragma unroll
    for (int j = 0; j < ASPLIT; ++j) MM = fmaxf(MM, gat[j * 34 + 32]);
    float o = 0.f, L = 0.f;
#pragma unroll
    for (int j = 0; j < ASPLIT; ++j) { const float wt = __builtin_amdgcn_exp2f((gat[j * 34 + 32] - MM) * LOG2E); o = fmaf(wt, gat[j * 34 + tid], o); L = fmaf(wt, gat[j * 34 + 33], L); }
    sv[tid] = bf16r(o / L);
  }
  __syncthreads();
  {
    float* red2 = red;
    const int chunk = tid % 80, rg = tid / 80;
    if (tid < 320) {
      float a[8];
#pragma unroll
      for (int k = 0; k < 8; ++k) a[k] = 0.f;
#pragma unroll
      for (int i = 0; i < 8; ++i) {
        const float sa = sv[rg * 8 + i];
        const u32x4 wa = *reinterpret_cast<const u32x4*>(lds + LA_WO + (rg * 8 + i) * 1280 + chunk * 16);
#pragma unroll
        for (int k = 0; k < 4; ++k) { a[2 * k] = fmaf(sa, bf_lo(wa[k]), a[2 * k]); a[2 * k + 1] = fmaf(sa, bf_hi(wa[k]), a[2 * k + 1]); }
      }
      *reinterpret_cast<f32x4*>(red2 + rg * 640 + chunk * 8) = f32x4{a[0], a[1], a[2], a[3]};
      *reinterpret_cast<f32x4*>(red2 + rg * 640 + chunk * 8 + 4) = f32x4{a[4], a[5], a[6], a[7]};
    }
    __syncthreads();
    if (tid < 160) {
      f32x4 sum = *reinterpret_cast<const f32x4*>(red2 + tid * 4);
#pragma unroll
      for (int g = 1; g < 4; ++g) sum += *reinterpret_cast<const f32x4*>(red2 + g * 640 + tid * 4);
      const unsigned off = WS_OG + (((tid >> 3) * NA + wg) * 32 + (tid & 7) * 4) * 8;
      st16_dev_u(rs, off, u32x4{f2u(sum[0]), 1u, f2u(sum[1]), 1u});
      st16_dev_u(rs, off + 16, u32x4{f2u(sum[2]), 1u, f2u(sum[3]), 1u});
    }
  }
  STAMP(6);
}

__device__ __forceinline__ void b_role(const Params& p, unsigned char* lds, int bb, int h, int half) {
  const u64 T = call_tag(p.ws);
  const int tid = threadIdx.x, wave = tid >> 6, lane = tid & 63, r = lane >> 3, c = lane & 7, r16 = lane & 15, g = lane >> 4;
  const __amdgpu_buffer_rsrc_t rs = ws_rsrc(p.ws);
  u32x4 cf[24];
  {
#pragma unroll
    for (int i = 0; i < ZPT; ++i) { const unsigned o = (unsigned)(bb * ZPER + tid + 512 * i); if (tid + 512 * i < ZPER) st16_dev_u(rs, WS_XG + o * 16, u32x4{0u, 0u, 0u, 0u}); }
    asm volatile("" ::: "memory");
    u32x4 w[6][3];
#pragma unroll
    for (int t = 0; t < 6; ++t) {
      const u16* q_ = p.Bwuk + (size_t)(384 * half + 48 * wave + 8 * t + r) * HD + h * DH + c * 8;
#pragma unroll
      for (int i = 0; i < 3; ++i) w[t][i] = ld16(q_ + i * 64);
    }
    asm volatile("s_waitcnt vmcnt(18)" ::: "memory");
    __syncthreads();
    if (tid == 64) set_flag(p.ws, WS_FZ, bb, T);
    unsigned qpk[12];
    gather_q(p.ws, 1, h, T, reinterpret_cast<unsigned*>(lds + L_Q), qpk);
    u16* sh = reinterpret_cast<u16*>(lds + L_RED);
#pragma unroll
    for (int t = 0; t < 6; ++t) {
      const float s = sum8(dot_row(w[t], qpk));
      const u16 hi = f2bf(s); const u16 lo = f2bf(s - bf2f(hi));
      if (c == 0) { sh[48 * wave + 8 * t + r] = hi; sh[384 + 48 * wave + 8 * t + r] = lo; }
    }
    __syncthreads();
    if (tid < 96) {
      const int part = tid / 48, ch = tid % 48;
      const u32x4 v = *reinterpret_cast<const u32x4*>(sh + part * 384 + ch * 8);
      st16_dev_u(rs, WS_QABS + ((part * H + h) * DC + 384 * half + ch * 8) * 2, v);
    }
    asm volatile("" :: "v"(T));
    publish(p.ws, WS_F1, bb, T);
  }
  STAMP(1);
  {
    u32x4 raw[24];
    const u16* cp = p.Bc + (size_t)(128 * bb + 16 * wave + r) * DC + 8 * c;
#pragma unroll
    for (int hf = 0; hf < 2; ++hf)
#pragma unroll
      for (int rg = 0; rg < 2; ++rg)
#pragma unroll
        for (int i = 0; i < 6; ++i) raw[hf * 12 + rg * 6 + i] = ld16(cp + (size_t)(8 * rg) * DC + 384 * hf + 64 * i);
#pragma unroll
    for (int hf = 0; hf < 2; ++hf) {
#pragma unroll
      for (int rg = 0; rg < 2; ++rg)
#pragma unroll
        for (int i = 0; i < 6; ++i)
          *reinterpret_cast<u32x4*>(lds + L_BIG + (16 * wave + 8 * rg + r) * CROW + (64 * i + 8 * c) * 2) = raw[hf * 12 + rg * 6 + i];
      asm volatile("s_waitcnt lgkmcnt(0)" ::: "memory");
#pragma unroll
      for (int s = 0; s < 12; ++s) cf[12 * hf + s] = *reinterpret_cast<const u32x4*>(lds + L_BIG + (16 * wave + r16) * CROW + (32 * s + 8 * g) * 2);
      asm volatile("s_waitcnt lgkmcnt(0)" ::: "memory");
    }
  }
  wait_flags(p.ws, WS_F1, T);
  STAMP(2);
#pragma unroll
  for (int i = 0; i < 12; ++i) {
    const int q = tid + 512 * i;
    const int row = q / 96, ch = q % 96;
    const u32x4 v = ld16_dev_u(rs, WS_QABS + q * 16);
    *reinterpret_cast<u32x4*>(lds + L_BIG + row * QROW + ch * 16) = v;
  }
  u32x4 wv[6][3];
#pragma unroll
  for (int t = 0; t < 6; ++t) {
    const u16* q_ = p.Bwuv + (size_t)(384 * half + 48 * wave + 8 * t + r) * HD + h * DH + c * 8;
#pragma unroll
    for (int i = 0; i < 3; ++i) wv[t][i] = ld16(q_ + i * 64);
  }
  __syncthreads();
  f32x4 sc[2] = {f32x4{0.f, 0.f, 0.f, 0.f}, f32x4{0.f, 0.f, 0.f, 0.f}};
#pragma unroll
  for (int s = 0; s < 24; ++s) {
#pragma unroll
    for (int ht = 0; ht < 2; ++ht) {
      const s16x8 bh = *reinterpret_cast<const s16x8*>(lds + L_BIG + (16 * ht + r16) * QROW + (32 * s + 8 * g) * 2);
      const s16x8 bl = *reinterpret_cast<const s16x8*>(lds + L_BIG + (32 + 16 * ht + r16) * QROW + (32 * s + 8 * g) * 2);
      sc[ht] = __builtin_amdgcn_mfma_f32_16x16x32_bf16(__builtin_bit_cast(s16x8, cf[s]), bh, sc[ht], 0, 0, 0);
      sc[ht] = __builtin_amdgcn_mfma_f32_16x16x32_bf16(__builtin_bit_cast(s16x8, cf[s]), bl, sc[ht], 0, 0, 0);
    }
  }
  float* wmax = reinterpret_cast<float*>(lds + L_ST);
  float* wsum = wmax + 256;
#pragma unroll
  for (int ht = 0; ht < 2; ++ht) {
#pragma unroll
    for (int k = 0; k < 4; ++k) sc[ht][k] = bf16r(bf16r(sc[ht][k]) * SCALE);
    float mx = fmaxf(fmaxf(sc[ht][0], sc[ht][1]), fmaxf(sc[ht][2], sc[ht][3]));
    mx = fmaxf(mx, __shfl_xor(mx, 16)); mx = fmaxf(mx, __shfl_xor(mx, 32));
    if (g == 0) wmax[wave * 32 + 16 * ht + r16] = mx;
  }
  __syncthreads();
#pragma unroll
  for (int ht = 0; ht < 2; ++ht) {
    float M = -INFINITY;
#pragma unroll
    for (int w8 = 0; w8 < 8; ++w8) M = fmaxf(M, wmax[w8 * 32 + 16 * ht + r16]);
    float e[4], sum = 0.f;
#pragma unroll
    for (int k = 0; k < 4; ++k) { e[k] = __builtin_amdgcn_exp2f((sc[ht][k] - M) * LOG2E); sum += e[k]; }
    sum += __shfl_xor(sum, 16); sum += __shfl_xor(sum, 32);
    if (g == 0) wsum[wave * 32 + 16 * ht + r16] = sum;
    u32x2 pk = {(unsigned)f2bf(e[0]) | ((unsigned)f2bf(e[1]) << 16), (unsigned)f2bf(e[2]) | ((unsigned)f2bf(e[3]) << 16)};
    *reinterpret_cast<u32x2*>(lds + L_P + (16 * ht + r16) * PROW + (16 * wave + 4 * g) * 2) = pk;
  }
#pragma unroll
  for (int s = 0; s < 12; ++s) *reinterpret_cast<u32x4*>(lds + L_BIG + (16 * wave + r16) * CROW + (32 * s + 8 * g) * 2) = cf[s];
  __syncthreads();
  if (tid < 32) {
    float M = -INFINITY, L = 0.f;
#pragma unroll
    for (int w8 = 0; w8 < 8; ++w8) { M = fmaxf(M, wmax[w8 * 32 + tid]); L += wsum[w8 * 32 + tid]; }
    st8_dev(p.ws, WS_BSTAT + (bb * H + tid) * 8, M, L);
  }
  {
    s16x8 pf[2][4];
#pragma unroll
    for (int ht = 0; ht < 2; ++ht)
#pragma unroll
      for (int ks = 0; ks < 4; ++ks) pf[ht][ks] = *reinterpret_cast<const s16x8*>(lds + L_P + (16 * ht + r16) * PROW + (32 * ks + 8 * g) * 2);
    const int q4 = r16 >> 2, p4 = r16 & 3;
#pragma unroll
    for (int ch = 0; ch < 2; ++ch) {
      if (ch == 1) {
        __syncthreads();
#pragma unroll
        for (int s = 0; s < 12; ++s) *reinterpret_cast<u32x4*>(lds + L_BIG + (16 * wave + r16) * CROW + (32 * s + 8 * g) * 2) = cf[12 + s];
        __syncthreads();
      }
#pragma unroll
      for (int ct = 0; ct < 3; ++ct) {
        f32x4 d[2] = {f32x4{0.f, 0.f, 0.f, 0.f}, f32x4{0.f, 0.f, 0.f, 0.f}};
#pragma unroll
        for (int ks = 0; ks < 4; ++ks) {
          const unsigned char* a0 = lds + L_BIG + (32 * ks + 8 * g + q4) * CROW + (48 * wave + 16 * ct + 4 * p4) * 2;
          typedef short v4i16_t __attribute__((ext_vector_type(4)));
          const v4i16_t t0 = __builtin_amdgcn_ds_read_tr16_b64_v4i16((LAS v4i16_t*)(a0));
          const v4i16_t t1 = __builtin_amdgcn_ds_read_tr16_b64_v4i16((LAS v4i16_t*)(a0 + 4 * CROW));
          const s16x8 af = {t0[0], t0[1], t0[2], t0[3], t1[0], t1[1], t1[2], t1[3]};
#pragma unroll
          for (int ht = 0; ht < 2; ++ht) d[ht] = __builtin_amdgcn_mfma_f32_16x16x32_bf16(af, pf[ht][ks], d[ht], 0, 0, 0);
        }
#pragma unroll
        for (int ht = 0; ht < 2; ++ht)
          st16_dev(rs, WS_BCTX + (unsigned)(((bb * H + 16 * ht + r16) * DC + 384 * ch + 48 * wave + 16 * ct + 4 * g) * 4), d[ht]);
      }
    }
  }
  publish(p.ws, WS_F2, bb, T);
  STAMP(3);
  if (wave >= 6) {
    const u16* wp = p.Bwo + (size_t)(h * DH + 96 * half) * DM;
    const int wv6 = __builtin_amdgcn_readfirstlane(wave) - 6;
#pragma unroll 4
    for (int i = 0; i < 60; ++i)
      __builtin_amdgcn_global_load_lds((const GAS void*)(wp + ((wv6 * 60 + i) * 64 + lane) * 8), (LAS void*)(lds + LB_WO + (wv6 * 60 + i) * 1024), 16, 0, 2);
  }
  if (wave < 6) flag_wait(p.ws, WS_FZ, T, flag_load(p.ws, WS_FZ));
  wait_flags(p.ws, WS_F2, T);
  STAMP(4);
  {
    float* coef = reinterpret_cast<float*>(lds + LB_COEF);
    float* part = reinterpret_cast<float*>(lds + LB_PART);
    float* red = reinterpret_cast<float*>(lds + LB_RED);
    float* cs = reinterpret_cast<float*>(lds + LB_CS);
    const int jg = tid / 96, q = tid % 96;
    f32x4 v[16];
    if (tid < 384) {
#pragma unroll
      for (int j = 0; j < 16; ++j) v[j] = ld16_dev(rs, WS_BCTX + (unsigned)((((16 * jg + j) * H + h) * DC + 384 * half + 4 * q) * 4));
    }
    if (tid < 64) {
      const f32x2 ml = ld8_dev(p.ws, WS_BSTAT + (tid * H + h) * 8);
      float M = ml[0];
#pragma unroll
      for (int o = 1; o < 64; o <<= 1) M = fmaxf(M, __shfl_xor(M, o));
      const float wt = __builtin_amdgcn_exp2f((ml[0] - M) * LOG2E);
      float L = wt * ml[1];
#pragma unroll
      for (int o = 1; o < 64; o <<= 1) L += __shfl_xor(L, o);
      coef[tid] = wt / L;
    }
    __syncthreads();
    if (tid < 384) {
      f32x4 a = {0.f, 0.f, 0.f, 0.f};
#pragma unroll
      for (int j = 0; j < 16; ++j) a += coef[16 * jg + j] * v[j];
      *reinterpret_cast<f32x4*>(part + jg * 384 + 4 * q) = a;
    }
    __syncthreads();
    float acc[24];
#pragma unroll
    for (int k = 0; k < 24; ++k) acc[k] = 0.f;
#pragma unroll
    for (int t = 0; t < 6; ++t) { const int row = 48 * wave + 8 * t + r; fma_row(acc, (part[row] + part[384 + row]) + (part[768 + row] + part[1152 + row]), wv[t]); }
#pragma unroll
    for (int k = 0; k < 24; ++k) { float x = acc[k]; x += dpp<ROR8>(x); x += __shfl_xor(x, 16); x += __shfl_xor(x, 32); acc[k] = x; }
    if (r == 0) {
#pragma unroll
      for (int i = 0; i < 3; ++i) {
        *reinterpret_cast<f32x4*>(red + wave * 192 + i * 64 + c * 8) = f32x4{acc[i * 8], acc[i * 8 + 1], acc[i * 8 + 2], acc[i * 8 + 3]};
        *reinterpret_cast<f32x4*>(red + wave * 192 + i * 64 + c * 8 + 4) = f32x4{acc[i * 8 + 4], acc[i * 8 + 5], acc[i * 8 + 6], acc[i * 8 + 7]};
      }
    }
    __syncthreads();
    if (tid < 192) {
      float s = 0.f;
#pragma unroll
      for (int w8 = 0; w8 < 8; ++w8) s += red[w8 * 192 + tid];
      cs[tid] = s;
      st_granule(p.ws, WS_XG + ((h * 2 + half) * DH + tid) * 8, granule(s));
    }
    STAMP(5);
    float other = 0.f;
    if (tid < 128) {
      const unsigned off = WS_XG + ((h * 2 + (half ^ 1)) * DH + 96 * half + (tid < 96 ? tid : 0)) * 8;
      unsigned long long a = 0;
      for (unsigned spins = 0; spins < (1u << 22); ++spins) {
        a = ld_granule(p.ws, off);
        if (__all((a >> 32) == 1ull)) break;
        __builtin_amdgcn_s_sleep(1);
      }
      other = __builtin_bit_cast(float, (unsigned)a);
    }
    asm volatile("s_waitcnt vmcnt(0)" ::: "memory");
    __syncthreads();
    if (tid < 96) { const float own = cs[96 * half + tid]; cs[192 + tid] = bf16r(half ? other + own : own + other); }
    __syncthreads();
    float* red2 = reinterpret_cast<float*>(lds + LB_RED2);
    const int chunk = tid % 80, rg = tid / 80;
    if (tid < 480) {
      float a[8];
#pragma unroll
      for (int k = 0; k < 8; ++k) a[k] = 0.f;
#pragma unroll
      for (int i = 0; i < 16; ++i) {
        const float s = cs[192 + rg * 16 + i];
        const u32x4 wo = *reinterpret_cast<const u32x4*>(lds + LB_WO + (rg * 16 + i) * 1280 + chunk * 16);
#pragma unroll
        for (int k = 0; k < 4; ++k) { a[2 * k] = fmaf(s, bf_lo(wo[k]), a[2 * k]); a[2 * k + 1] = fmaf(s, bf_hi(wo[k]), a[2 * k + 1]); }
      }
      *reinterpret_cast<f32x4*>(red2 + rg * 640 + chunk * 8) = f32x4{a[0], a[1], a[2], a[3]};
      *reinterpret_cast<f32x4*>(red2 + rg * 640 + chunk * 8 + 4) = f32x4{a[4], a[5], a[6], a[7]};
    }
    __syncthreads();
    if (tid < 160) {
      f32x4 sum = *reinterpret_cast<const f32x4*>(red2 + tid * 4);
#pragma unroll
      for (int g = 1; g < 6; ++g) sum += *reinterpret_cast<const f32x4*>(red2 + g * 640 + tid * 4);
      const unsigned off = WS_OGB + (((tid >> 3) * NB + bb) * 32 + (tid & 7) * 4) * 8;
      st16_dev_u(rs, off, u32x4{f2u(sum[0]), 1u, f2u(sum[1]), 1u});
      st16_dev_u(rs, off + 16, u32x4{f2u(sum[2]), 1u, f2u(sum[3]), 1u});
    }
  }
  STAMP(6);
  if (bb < 40) {
    float* buf = reinterpret_cast<float*>(lds + L_BIG);
    float* red = reinterpret_cast<float*>(lds + LB_RED2);
    const int np = bb < 20 ? 6 : 2;
    const unsigned gbase = bb < 20 ? WS_OG + bb * NA * 256 : WS_OGB + (bb - 20) * NB * 256;
    if (wave >= 6) flag_wait(p.ws, WS_FZ, T, flag_load(p.ws, WS_FZ));
    __syncthreads();
    {
      const int j = tid >> 4, q = tid & 15;
      u32x4 v[6];
      for (unsigned spins = 0; spins < (1u << 22); ++spins) {
        bool ok = true;
#pragma unroll
        for (int i = 0; i < 6; ++i) { v[i] = ld16_dev_u(rs, gbase + ((j + 32 * (i < np ? i : 0)) * 32 + 2 * q) * 8); ok = ok && v[i][1] == 1u && v[i][3] == 1u; }
        if (__all(ok)) break;
        __builtin_amdgcn_s_sleep(4);
      }
#pragma unroll
      for (int i = 0; i < 6; ++i) { buf[(j + 32 * i) * 32 + 2 * q] = i < np ? u2f(v[i][0]) : 0.f; buf[(j + 32 * i) * 32 + 2 * q + 1] = i < np ? u2f(v[i][2]) : 0.f; }
    }
    STAMP(7);
    __syncthreads();
    if (tid < 256) {
      const int part = tid >> 5, col = tid & 31; float s = 0.f;
#pragma unroll
      for (int j = 0; j < 24; ++j) s += buf[(part * 24 + j) * 32 + col];
      red[tid] = s;
    }
    __syncthreads();
    if (tid < 32) {
      float s = 0.f;
#pragma unroll
      for (int j = 0; j < 8; ++j) s += red[j * 32 + tid];
      p.out[32 * bb + tid] = f2bf(s);
    }
    if (bb == 0 && tid == 0) {
      GAS u64* e = (GAS u64*)(p.ws + WS_EPOCH);
      __hip_atomic_store(e, __hip_atomic_load(e, __ATOMIC_RELAXED, __HIP_MEMORY_SCOPE_AGENT) + 1ull, __ATOMIC_RELAXED, __HIP_MEMORY_SCOPE_AGENT);
    }
  }
  STAMP(8);
}

__global__ void __launch_bounds__(NTHR, 2) fused_decode_kernel(Params p) {
  __shared__ __attribute__((aligned(16))) unsigned char lds[LDS_BYTES];
  const int b = blockIdx.x;
  STAMP(0);
  if (b < NA) {
    const int x = b & 7, idx = b >> 3;
    const int h = x * 4 + idx / ASPLIT, split = idx % ASPLIT;
    a_role(p, lds, h * ASPLIT + split, h, split);
  } else {
    const int bb = b - NA;
    b_role(p, lds, bb, (bb & 7) * 4 + (bb >> 4), (bb >> 3) & 1);
  }
}

extern "C" void kernel_launch(void* const* d_in, const int* in_sizes, int n_in,
                              void* d_out, int out_size, void* d_ws, size_t ws_size,
                              hipStream_t stream) {
  Params p;
  p.Ax = (const u16*)d_in[0]; p.Ak = (const u16*)d_in[1]; p.Av = (const u16*)d_in[2]; p.Awq = (const u16*)d_in[3]; p.Awo = (const u16*)d_in[4];
  p.Bx = (const u16*)d_in[5]; p.Bc = (const u16*)d_in[6]; p.Bwq = (const u16*)d_in[7]; p.Bwuk = (const u16*)d_in[8]; p.Bwuv = (const u16*)d_in[9]; p.Bwo = (const u16*)d_in[10];
  p.out = (u16*)d_out; p.ws = (unsigned char*)d_ws;
  fused_decode_kernel<<<NA + NB, NTHR, 0, stream>>>(p);
}
```

```cpp
#include <hip/hip_runtime.h>
#include <hip/hip_bf16.h>
#include <stdint.h>

#define GAS __attribute__((address_space(1)))
#define LAS __attribute__((address_space(3)))
typedef unsigned u32x4 __attribute__((ext_vector_type(4)));
typedef unsigned u32x2 __attribute__((ext_vector_type(2)));
typedef float f32x4 __attribute__((ext_vector_type(4)));
typedef float f32x2 __attribute__((ext_vector_type(2)));
typedef short s16x8 __attribute__((ext_vector_type(8)));
typedef short s16x4 __attribute__((ext_vector_type(4)));
typedef __bf16 bf16x2_t __attribute__((ext_vector_type(2)));
typedef unsigned short u16;

constexpr int H = 32, SK = 8192, DH = 192, DM = 640, DC = 768, HD = H * DH;
constexpr int NA = 192, NB = 64, NTHR = 512, ASPLIT = 6;
constexpr float SCALE = 0.07216878364870322f;
constexpr float LOG2E = 1.4426950408889634f;

constexpr unsigned WS_EPOCH = 0;
constexpr unsigned WS_FZ = 1024, WS_F1 = 2048, WS_F2 = 3072;
constexpr unsigned WS_QG = 4096;
constexpr unsigned WS_XG = 131072;
constexpr unsigned WS_PG = WS_XG + H * 2 * DH * 8;
constexpr unsigned WS_OG = WS_PG + NA * 200 * 8;
constexpr unsigned WS_OGB = WS_OG + 20 * NA * 32 * 8;
constexpr unsigned WS_QABS = WS_OGB + 20 * NB * 32 * 8;
constexpr unsigned WS_BSTAT = WS_QABS + 2 * H * DC * 2;
constexpr unsigned WS_BCTX = WS_BSTAT + NB * H * 2 * 4;
constexpr unsigned WS_END = WS_BCTX + NB * H * DC * 4;
static_assert(WS_QG + 4 * 3072 * 8 <= WS_XG, "ws");
constexpr int ZPER = (int)((WS_QABS - WS_XG) / 16 / NB), ZPT = (ZPER + 511) / 512;
static_assert((WS_QABS - WS_XG) % (16 * NB) == 0, "ws");

constexpr int QROW = 1552;
constexpr int CROW = 784;
constexpr int PROW = 272;
constexpr int L_BIG = 0;
constexpr int L_P = 100352;
constexpr int L_ST = L_P + 32 * PROW;
constexpr int L_RED = L_ST + 2048;
constexpr int L_Q = L_RED + 16384;
constexpr int LB_WO = 0, LB_PART = 122880, LB_COEF = LB_PART + 6144, LB_RED = LB_COEF + 512, LB_CS = LB_RED + 6144, LB_RED2 = LB_CS + 1536;
constexpr int LDS_BYTES = LB_RED2 + 15360;
static_assert(L_Q + 1024 <= LDS_BYTES, "lds");
constexpr int LA_WO = 0;
constexpr int LA_RED = 40960;
constexpr int LA_Q = LA_RED + 24576;
static_assert(LA_Q + 4096 <= LDS_BYTES, "lds");

struct Params {
  const u16* Ax; const u16* Ak; const u16* Av; const u16* Awq; const u16* Awo;
  const u16* Bx; const u16* Bc; const u16* Bwq; const u16* Bwuk; const u16* Bwuv; const u16* Bwo;
  u16* out; unsigned char* ws;
};

__device__ __forceinline__ unsigned f2u(float x) { return __builtin_bit_cast(unsigned, x); }
__device__ __forceinline__ float u2f(unsigned x) { return __builtin_bit_cast(float, x); }
__device__ __forceinline__ float bf_lo(unsigned u) { return __builtin_bit_cast(float, u << 16); }
__device__ __forceinline__ float bf_hi(unsigned u) { return __builtin_bit_cast(float, u & 0xffff0000u); }
__device__ __forceinline__ float bf2f(u16 u) { return __builtin_bit_cast(float, (unsigned)u << 16); }
__device__ __forceinline__ u16 f2bf(float x) { __bf16 b = (__bf16)x; return __builtin_bit_cast(u16, b); }
__device__ __forceinline__ float bf16r(float x) { return bf2f(f2bf(x)); }
__device__ __forceinline__ float dot2(unsigned a, unsigned b, float acc) {
  return __builtin_amdgcn_fdot2_f32_bf16(__builtin_bit_cast(bf16x2_t, a), __builtin_bit_cast(bf16x2_t, b), acc, false);
}
template <int CTRL> __device__ __forceinline__ float dpp(float x) {
  return __builtin_bit_cast(float, __builtin_amdgcn_mov_dpp(__builtin_bit_cast(int, x), CTRL, 0xf, 0xf, true));
}
constexpr int XOR1 = 0xB1, XOR2 = 0x4E, HMIRROR = 0x141, ROR8 = 0x128, ROR4 = 0x124;
__device__ __forceinline__ float sum8(float s) {
  s += dpp<XOR1>(s); s += dpp<XOR2>(s); s += dpp<HMIRROR>(s); return s;
}
__device__ __forceinline__ u32x4 ld16(const u16* p) { return __builtin_nontemporal_load(reinterpret_cast<const u32x4*>(p)); }

__device__ __forceinline__ __amdgpu_buffer_rsrc_t ws_rsrc(unsigned char* ws) {
  return __builtin_amdgcn_make_buffer_rsrc(ws, 0, (int)WS_END, 0x00020000);
}
__device__ __forceinline__ void st16_dev(__amdgpu_buffer_rsrc_t r, unsigned off, f32x4 v) {
  __builtin_amdgcn_raw_buffer_store_b128(__builtin_bit_cast(u32x4, v), r, off, 0, 16);
}
__device__ __forceinline__ void st16_dev_u(__amdgpu_buffer_rsrc_t r, unsigned off, u32x4 v) {
  __builtin_amdgcn_raw_buffer_store_b128(v, r, off, 0, 16);
}
__device__ __forceinline__ f32x4 ld16_dev(__amdgpu_buffer_rsrc_t r, unsigned off) {
  return __builtin_bit_cast(f32x4, __builtin_amdgcn_raw_buffer_load_b128(r, off, 0, 16));
}
__device__ __forceinline__ u32x4 ld16_dev_u(__amdgpu_buffer_rsrc_t r, unsigned off) {
  return __builtin_amdgcn_raw_buffer_load_b128(r, off, 0, 16);
}
__device__ __forceinline__ void st4_dev(unsigned char* ws, unsigned off, float v) {
  __hip_atomic_store((GAS float*)(ws + off), v, __ATOMIC_RELAXED, __HIP_MEMORY_SCOPE_AGENT);
}
__device__ __forceinline__ void st4_dev_u(unsigned char* ws, unsigned off, unsigned v) {
  __hip_atomic_store((GAS unsigned*)(ws + off), v, __ATOMIC_RELAXED, __HIP_MEMORY_SCOPE_AGENT);
}
__device__ __forceinline__ float ld4_dev(unsigned char* ws, unsigned off) {
  return __hip_atomic_load((GAS float*)(ws + off), __ATOMIC_RELAXED, __HIP_MEMORY_SCOPE_AGENT);
}
__device__ __forceinline__ void st8_dev(unsigned char* ws, unsigned off, float a, float b) {
  f32x2 v = {a, b};
  __hip_atomic_store((GAS unsigned long long*)(ws + off), __builtin_bit_cast(unsigned long long, v), __ATOMIC_RELAXED, __HIP_MEMORY_SCOPE_AGENT);
}
__device__ __forceinline__ f32x2 ld8_dev(unsigned char* ws, unsigned off) {
  unsigned long long v = __hip_atomic_load((GAS unsigned long long*)(ws + off), __ATOMIC_RELAXED, __HIP_MEMORY_SCOPE_AGENT);
  return __builtin_bit_cast(f32x2, v);
}
typedef unsigned long long u64;
__device__ __forceinline__ u64 call_tag(unsigned char* ws) {
  u64 z = __hip_atomic_load((const GAS u64*)(ws + WS_EPOCH), __ATOMIC_RELAXED, __HIP_MEMORY_SCOPE_AGENT) + 1ull;
  z *= 0x9E3779B97F4A7C15ull;
  z = (z ^ (z >> 30)) * 0xBF58476D1CE4E5B9ull; z = (z ^ (z >> 27)) * 0x94D049BB133111EBull;
  return z ^ (z >> 31);
}
__device__ __forceinline__ void set_flag(unsigned char* ws, unsigned base, int i, u64 T) {
  __hip_atomic_store((GAS u64*)(ws + base + i * 8), T, __ATOMIC_RELAXED, __HIP_MEMORY_SCOPE_AGENT);
}
__device__ __forceinline__ u64 flag_load(unsigned char* ws, unsigned base) {
  return __hip_atomic_load((const GAS u64*)(ws + base + (threadIdx.x & 63) * 8), __ATOMIC_RELAXED, __HIP_MEMORY_SCOPE_AGENT);
}
__device__ __forceinline__ void flag_wait(unsigned char* ws, unsigned base, u64 T, u64 z) {
  for (unsigned spins = 0; spins < (1u << 22); ++spins) {
    if (__all(z == T)) break;
    __builtin_amdgcn_s_sleep(2);
    z = flag_load(ws, base);
  }
}
__device__ __forceinline__ void publish(unsigned char* ws, unsigned base, int i, u64 T) {
  asm volatile("s_waitcnt vmcnt(0)" ::: "memory");
  __syncthreads();
  if (threadIdx.x == 0) set_flag(ws, base, i, T);
}
__device__ __forceinline__ void wait_flags(unsigned char* ws, unsigned base, u64 T) {
  if (threadIdx.x < 64) flag_wait(ws, base, T, flag_load(ws, base));
  __syncthreads();
}

__device__ __forceinline__ unsigned long long granule(float v) { return (1ull << 32) | (unsigned long long)__builtin_bit_cast(unsigned, v); }
__device__ __forceinline__ void st_granule(unsigned char* ws, unsigned off, unsigned long long gv) {
  __hip_atomic_store((GAS unsigned long long*)(ws + off), gv, __ATOMIC_RELAXED, __HIP_MEMORY_SCOPE_AGENT);
}
__device__ __forceinline__ unsigned long long ld_granule(unsigned char* ws, unsigned off) {
  return __hip_atomic_load((const GAS unsigned long long*)(ws + off), __ATOMIC_RELAXED, __HIP_MEMORY_SCOPE_AGENT);
}
__device__ __forceinline__ void gather_q(unsigned char* ws, int which, int h, u64 T, unsigned* lq, unsigned (&qpk)[12]) {
  const int tid = threadIdx.x, c = tid & 7;
  if (tid < 64) {
    const unsigned goff = WS_QG + (which * 3072 + h * 96 + 2 * (tid < 48 ? tid : 0)) * 8;
    const u64 t1 = T & 0xffffffffull, t2 = T >> 32;
    u64 a = 0, b = 0;
    for (unsigned spins = 0; spins < (1u << 22); ++spins) {
      a = ld_granule(ws, goff);
      b = ld_granule(ws, goff + 8);
      const u64 a2 = ld_granule(ws, goff + 2 * 3072 * 8), b2 = ld_granule(ws, goff + 2 * 3072 * 8 + 8);
      if (__all((a >> 32) == t1 && (b >> 32) == t1 && (a2 >> 32) == t2 && (b2 >> 32) == t2 && (unsigned)a == (unsigned)a2 && (unsigned)b == (unsigned)b2)) break;
      __builtin_amdgcn_s_sleep(2);
    }
    if (tid < 48) { lq[2 * tid] = (unsigned)a; lq[2 * tid + 1] = (unsigned)b; }
  }
  __syncthreads();
#pragma unroll
  for (int i = 0; i < 3; ++i)
#pragma unroll
    for (int k = 0; k < 4; ++k) qpk[i * 4 + k] = lq[i * 32 + c * 4 + k];
}

__device__ __forceinline__ void fma_row(float (&acc)[24], float s, const u32x4 (&w)[3]) {
#pragma unroll
  for (int i = 0; i < 3; ++i)
#pragma unroll
    for (int k = 0; k < 4; ++k) {
      acc[i * 8 + 2 * k] = fmaf(s, bf_lo(w[i][k]), acc[i * 8 + 2 * k]);
      acc[i * 8 + 2 * k + 1] = fmaf(s, bf_hi(w[i][k]), acc[i * 8 + 2 * k + 1]);
    }
}
__device__ __forceinline__ float dot_row(const u32x4 (&w)[3], const unsigned (&q)[12]) {
  float s0 = 0.f, s1 = 0.f, s2 = 0.f;
#pragma unroll
  for (int k = 0; k < 4; ++k) { s0 = dot2(w[0][k], q[k], s0); s1 = dot2(w[1][k], q[4 + k], s1); s2 = dot2(w[2][k], q[8 + k], s2); }
  return (s0 + s1) + s2;
}
__device__ __forceinline__ void a_role(const Params& p, unsigned char* lds, int wg, int h, int split) {
  const u64 T = call_tag(p.ws);
  const int tid = threadIdx.x, wave = __builtin_amdgcn_readfirstlane(tid >> 6), lane = tid & 63, r = lane >> 3, c = lane & 7;
  const int gw = split * 8 + wave;
  const int t0 = (1024 * gw) / 48, t1 = (1024 * (gw + 1)) / 48, nt = t1 - t0;
  const u16* kb = p.Ak + ((size_t)h * SK + r) * DH + c * 8;
  const u16* vb = p.Av + ((size_t)h * SK + r) * DH + c * 8;
  const __amdgpu_buffer_rsrc_t rs = ws_rsrc(p.ws);
  u32x4 kr[4][3], vr[4][3];
  unsigned qpk[12];
  float* red = reinterpret_cast<float*>(lds + LA_RED);
#define A_LOAD(slot, idx) do { const int tt_ = t0 + ((idx) < nt ? (idx) : nt - 1); const size_t o_ = (size_t)tt_ * 8 * DH; \
    _Pragma("unroll") for (int i_ = 0; i_ < 3; ++i_) kr[slot][i_] = ld16(kb + o_ + i_ * 64); \
    _Pragma("unroll") for (int i_ = 0; i_ < 3; ++i_) vr[slot][i_] = ld16(vb + o_ + i_ * 64); } while (0)
  {
    const int which = split >= 3 ? 1 : 0, sl = split - 3 * which;
    const u16* x = which ? p.Bx : p.Ax;
    const u16* W = (which ? p.Bwq : p.Awq) + h * DH + 64 * sl + c * 8;
    float xa[10]; u32x4 wa[10];
#pragma unroll
    for (int t = 0; t < 10; ++t) xa[t] = bf2f(x[wave * 80 + t * 8 + r]);
#pragma unroll
    for (int t = 0; t < 10; ++t) wa[t] = ld16(W + (size_t)(wave * 80 + t * 8 + r) * HD);
#pragma unroll
    for (int d = 0; d < 4; ++d) A_LOAD(d, d);
    float a[8];
#pragma unroll
    for (int k = 0; k < 8; ++k) a[k] = 0.f;
#pragma unroll
    for (int t = 0; t < 10; ++t)
#pragma unroll
      for (int k = 0; k < 4; ++k) { a[2 * k] = fmaf(xa[t], bf_lo(wa[t][k]), a[2 * k]); a[2 * k + 1] = fmaf(xa[t], bf_hi(wa[t][k]), a[2 * k + 1]); }
#pragma unroll
    for (int k = 0; k < 8; ++k) { float v = a[k]; v += dpp<ROR8>(v); v += __shfl_xor(v, 16); v += __shfl_xor(v, 32); a[k] = v; }
    if (r == 0) {
      *reinterpret_cast<f32x4*>(red + wave * 64 + c * 8) = f32x4{a[0], a[1], a[2], a[3]};
      *reinterpret_cast<f32x4*>(red + wave * 64 + c * 8 + 4) = f32x4{a[4], a[5], a[6], a[7]};
    }
    asm volatile("" :: "v"(T));
    __syncthreads();
    if (tid < 32) {
      float s0 = 0.f, s1 = 0.f;
#pragma unroll
      for (int w8 = 0; w8 < 8; ++w8) { s0 += red[w8 * 64 + 2 * tid]; s1 += red[w8 * 64 + 2 * tid + 1]; }
      const u64 qv = (u64)((unsigned)f2bf(s0) | ((unsigned)f2bf(s1) << 16));
      st_granule(p.ws, WS_QG + (which * 3072 + h * 96 + 32 * sl + tid) * 8, (T << 32) | qv);
      st_granule(p.ws, WS_QG + (2 * 3072 + which * 3072 + h * 96 + 32 * sl + tid) * 8, (T & 0xffffffff00000000ull) | qv);
    }
  }
  gather_q(p.ws, 0, h, T, reinterpret_cast<unsigned*>(lds + LA_Q), qpk);
  float m = -INFINITY, l = 0.f, acc[24];
#pragma unroll
  for (int k = 0; k < 24; ++k) acc[k] = 0.f;
#define A_STEP(slot, idx) do { \
    float s_ = sum8(dot_row(kr[slot], qpk)); \
    s_ = bf16r(bf16r(s_) * SCALE); \
    s_ = ((idx) < nt) ? s_ : -INFINITY; \
    const float mn_ = fmaxf(m, s_); \
    const float al_ = __builtin_amdgcn_exp2f((m - mn_) * LOG2E), pp_ = __builtin_amdgcn_exp2f((s_ - mn_) * LOG2E); \
    l = fmaf(l, al_, pp_); m = mn_; \
    _Pragma("unroll") for (int k_ = 0; k_ < 24; ++k_) acc[k_] *= al_; \
    fma_row(acc, pp_, vr[slot]); } while (0)
  for (int it = 0; it < 5; ++it) {
#pragma unroll
    for (int d = 0; d < 4; ++d) { A_STEP(d, it * 4 + d); A_LOAD(d, it * 4 + d + 4); }
  }
  const u64 zc = flag_load(p.ws, WS_FZ);
  u32x4 wot[5];
#pragma unroll
  for (int i = 0; i < 5; ++i) wot[i] = ld16(p.Awo + (size_t)(h * DH + 32 * split) * DM + (tid + 512 * i) * 8);
#pragma unroll
  for (int d = 0; d < 4; ++d) A_STEP(d, 20 + d);
  flag_wait(p.ws, WS_FZ, T, zc);
#pragma unroll
  for (int i = 0; i < 5; ++i) *reinterpret_cast<u32x4*>(lds + LA_WO + (tid + 512 * i) * 16) = wot[i];
#undef A_STEP
#undef A_LOAD
  {
    float M = m; M = fmaxf(M, dpp<ROR8>(M)); M = fmaxf(M, __shfl_xor(M, 16)); M = fmaxf(M, __shfl_xor(M, 32));
    const float f = __builtin_amdgcn_exp2f((m - M) * LOG2E);
    l *= f; l += dpp<ROR8>(l); l += __shfl_xor(l, 16); l += __shfl_xor(l, 32);
#pragma unroll
    for (int k = 0; k < 24; ++k) { float v = acc[k] * f; v += dpp<ROR8>(v); v += __shfl_xor(v, 16); v += __shfl_xor(v, 32); acc[k] = v; }
    if (r == 0) {
#pragma unroll
      for (int i = 0; i < 3; ++i) {
        *reinterpret_cast<f32x4*>(red + wave * 200 + i * 64 + c * 8) = f32x4{acc[i * 8], acc[i * 8 + 1], acc[i * 8 + 2], acc[i * 8 + 3]};
        *reinterpret_cast<f32x4*>(red + wave * 200 + i * 64 + c * 8 + 4) = f32x4{acc[i * 8 + 4], acc[i * 8 + 5], acc[i * 8 + 6], acc[i * 8 + 7]};
      }
      if (c == 0) { red[wave * 200 + 192] = M; red[wave * 200 + 193] = l; }
    }
    __syncthreads();
    if (tid < 194) {
      float MM = -INFINITY;
#pragma unroll
      for (int w8 = 0; w8 < 8; ++w8) MM = fmaxf(MM, red[w8 * 200 + 192]);
      float o = 0.f;
      const int src = tid < 192 ? tid : 193;
#pragma unroll
      for (int w8 = 0; w8 < 8; ++w8) o = fmaf(__builtin_amdgcn_exp2f((red[w8 * 200 + 192] - MM) * LOG2E), red[w8 * 200 + src], o);
      st_granule(p.ws, WS_PG + (wg * 200 + tid) * 8, granule(tid == 192 ? MM : o));
    }
  }
  float* gat = reinterpret_cast<float*>(lds + LA_Q);
  float* sv = gat + 512;
  if (tid < 256) {
    const int t_ = tid < 204 ? tid : 0, j = t_ / 34, k = t_ % 34;
    const unsigned off = WS_PG + ((h * ASPLIT + j) * 200 + (k < 32 ? 32 * split + k : 160 + k)) * 8;
    unsigned long long a = 0;
    for (unsigned spins = 0; spins < (1u << 22); ++spins) {
      a = ld_granule(p.ws, off);
      if (__all((a >> 32) == 1ull)) break;
      __builtin_amdgcn_s_sleep(1);
    }
    gat[tid] = __builtin_bit_cast(float, (unsigned)a);
  }
  __syncthreads();
  if (tid < 32) {
    float MM = -INFINITY;
#pragma unroll
    for (int j = 0; j < ASPLIT; ++j) MM = fmaxf(MM, gat[j * 34 + 32]);
    float o = 0.f, L = 0.f;
#pragma unroll
    for (int j = 0; j < ASPLIT; ++j) { const float wt = __builtin_amdgcn_exp2f((gat[j * 34 + 32] - MM) * LOG2E); o = fmaf(wt, gat[j * 34 + tid], o); L = fmaf(wt, gat[j * 34 + 33], L); }
    sv[tid] = bf16r(o / L);
  }
  __syncthreads();
  {
    float* red2 = red;
    const int chunk = tid % 80, rg = tid / 80;
    if (tid < 320) {
      float a[8];
#pragma unroll
      for (int k = 0; k < 8; ++k) a[k] = 0.f;
#pragma unroll
      for (int i = 0; i < 8; ++i) {
        const float sa = sv[rg * 8 + i];
        const u32x4 wa = *reinterpret_cast<const u32x4*>(lds + LA_WO + (rg * 8 + i) * 1280 + chunk * 16);
#pragma unroll
        for (int k = 0; k < 4; ++k) { a[2 * k] = fmaf(sa, bf_lo(wa[k]), a[2 * k]); a[2 * k + 1] = fmaf(sa, bf_hi(wa[k]), a[2 * k + 1]); }
      }
      *reinterpret_cast<f32x4*>(red2 + rg * 640 + chunk * 8) = f32x4{a[0], a[1], a[2], a[3]};
      *reinterpret_cast<f32x4*>(red2 + rg * 640 + chunk * 8 + 4) = f32x4{a[4], a[5], a[6], a[7]};
    }
    __syncthreads();
    if (tid < 160) {
      f32x4 sum = *reinterpret_cast<const f32x4*>(red2 + tid * 4);
#pragma unroll
      for (int g = 1; g < 4; ++g) sum += *reinterpret_cast<const f32x4*>(red2 + g * 640 + tid * 4);
      const unsigned off = WS_OG + (((tid >> 3) * NA + wg) * 32 + (tid & 7) * 4) * 8;
      st16_dev_u(rs, off, u32x4{f2u(sum[0]), 1u, f2u(sum[1]), 1u});
      st16_dev_u(rs, off + 16, u32x4{f2u(sum[2]), 1u, f2u(sum[3]), 1u});
    }
  }
}

__device__ __forceinline__ void b_role(const Params& p, unsigned char* lds, int bb, int h, int half) {
  const u64 T = call_tag(p.ws);
  const int tid = threadIdx.x, wave = tid >> 6, lane = tid & 63, r = lane >> 3, c = lane & 7, r16 = lane & 15, g = lane >> 4;
  const __amdgpu_buffer_rsrc_t rs = ws_rsrc(p.ws);
  u32x4 cf[24];
  {
#pragma unroll
    for (int i = 0; i < ZPT; ++i) { const unsigned o = (unsigned)(bb * ZPER + tid + 512 * i); if (tid + 512 * i < ZPER) st16_dev_u(rs, WS_XG + o * 16, u32x4{0u, 0u, 0u, 0u}); }
    asm volatile("" ::: "memory");
    u32x4 w[6][3];
#pragma unroll
    for (int t = 0; t < 6; ++t) {
      const u16* q_ = p.Bwuk + (size_t)(384 * half + 48 * wave + 8 * t + r) * HD + h * DH + c * 8;
#pragma unroll
      for (int i = 0; i < 3; ++i) w[t][i] = ld16(q_ + i * 64);
    }
    asm volatile("s_waitcnt vmcnt(18)" ::: "memory");
    __syncthreads();
    if (tid == 64) set_flag(p.ws, WS_FZ, bb, T);
    unsigned qpk[12];
    gather_q(p.ws, 1, h, T, reinterpret_cast<unsigned*>(lds + L_Q), qpk);
    u16* sh = reinterpret_cast<u16*>(lds + L_RED);
#pragma unroll
    for (int t = 0; t < 6; ++t) {
      const float s = sum8(dot_row(w[t], qpk));
      const u16 hi = f2bf(s); const u16 lo = f2bf(s - bf2f(hi));
      if (c == 0) { sh[48 * wave + 8 * t + r] = hi; sh[384 + 48 * wave + 8 * t + r] = lo; }
    }
    __syncthreads();
    if (tid < 96) {
      const int part = tid / 48, ch = tid % 48;
      const u32x4 v = *reinterpret_cast<const u32x4*>(sh + part * 384 + ch * 8);
      st16_dev_u(rs, WS_QABS + ((part * H + h) * DC + 384 * half + ch * 8) * 2, v);
    }
    asm volatile("" :: "v"(T));
    publish(p.ws, WS_F1, bb, T);
  }
  {
    u32x4 raw[24];
    const u16* cp = p.Bc + (size_t)(128 * bb + 16 * wave + r) * DC + 8 * c;
#pragma unroll
    for (int hf = 0; hf < 2; ++hf)
#pragma unroll
      for (int rg = 0; rg < 2; ++rg)
#pragma unroll
        for (int i = 0; i < 6; ++i) raw[hf * 12 + rg * 6 + i] = ld16(cp + (size_t)(8 * rg) * DC + 384 * hf + 64 * i);
#pragma unroll
    for (int hf = 0; hf < 2; ++hf) {
#pragma unroll
      for (int rg = 0; rg < 2; ++rg)
#pragma unroll
        for (int i = 0; i < 6; ++i)
          *reinterpret_cast<u32x4*>(lds + L_BIG + (16 * wave + 8 * rg + r) * CROW + (64 * i + 8 * c) * 2) = raw[hf * 12 + rg * 6 + i];
      asm volatile("s_waitcnt lgkmcnt(0)" ::: "memory");
#pragma unroll
      for (int s = 0; s < 12; ++s) cf[12 * hf + s] = *reinterpret_cast<const u32x4*>(lds + L_BIG + (16 * wave + r16) * CROW + (32 * s + 8 * g) * 2);
      asm volatile("s_waitcnt lgkmcnt(0)" ::: "memory");
    }
  }
  wait_flags(p.ws, WS_F1, T);
#pragma unroll
  for (int i = 0; i < 12; ++i) {
    const int q = tid + 512 * i;
    const int row = q / 96, ch = q % 96;
    const u32x4 v = ld16_dev_u(rs, WS_QABS + q * 16);
    *reinterpret_cast<u32x4*>(lds + L_BIG + row * QROW + ch * 16) = v;
  }
  u32x4 wv[6][3];
#pragma unroll
  for (int t = 0; t < 6; ++t) {
    const u16* q_ = p.Bwuv + (size_t)(384 * half + 48 * wave + 8 * t + r) * HD + h * DH + c * 8;
#pragma unroll
    for (int i = 0; i < 3; ++i) wv[t][i] = ld16(q_ + i * 64);
  }
  __syncthreads();
  f32x4 sc[2] = {f32x4{0.f, 0.f, 0.f, 0.f}, f32x4{0.f, 0.f, 0.f, 0.f}};
#pragma unroll
  for (int s = 0; s < 24; ++s) {
#pragma unroll
    for (int ht = 0; ht < 2; ++ht) {
      const s16x8 bh = *reinterpret_cast<const s16x8*>(lds + L_BIG + (16 * ht + r16) * QROW + (32 * s + 8 * g) * 2);
      const s16x8 bl = *reinterpret_cast<const s16x8*>(lds + L_BIG + (32 + 16 * ht + r16) * QROW + (32 * s + 8 * g) * 2);
      sc[ht] = __builtin_amdgcn_mfma_f32_16x16x32_bf16(__builtin_bit_cast(s16x8, cf[s]), bh, sc[ht], 0, 0, 0);
      sc[ht] = __builtin_amdgcn_mfma_f32_16x16x32_bf16(__builtin_bit_cast(s16x8, cf[s]), bl, sc[ht], 0, 0, 0);
    }
  }
  float* wmax = reinterpret_cast<float*>(lds + L_ST);
  float* wsum = wmax + 256;
#pragma unroll
  for (int ht = 0; ht < 2; ++ht) {
#pragma unroll
    for (int k = 0; k < 4; ++k) sc[ht][k] = bf16r(bf16r(sc[ht][k]) * SCALE);
    float mx = fmaxf(fmaxf(sc[ht][0], sc[ht][1]), fmaxf(sc[ht][2], sc[ht][3]));
    mx = fmaxf(mx, __shfl_xor(mx, 16)); mx = fmaxf(mx, __shfl_xor(mx, 32));
    if (g == 0) wmax[wave * 32 + 16 * ht + r16] = mx;
  }
  __syncthreads();
#pragma unroll
  for (int ht = 0; ht < 2; ++ht) {
    float M = -INFINITY;
#pragma unroll
    for (int w8 = 0; w8 < 8; ++w8) M = fmaxf(M, wmax[w8 * 32 + 16 * ht + r16]);
    float e[4], sum = 0.f;
#pragma unroll
    for (int k = 0; k < 4; ++k) { e[k] = __builtin_amdgcn_exp2f((sc[ht][k] - M) * LOG2E); sum += e[k]; }
    sum += __shfl_xor(sum, 16); sum += __shfl_xor(sum, 32);
    if (g == 0) wsum[wave * 32 + 16 * ht + r16] = sum;
    u32x2 pk = {(unsigned)f2bf(e[0]) | ((unsigned)f2bf(e[1]) << 16), (unsigned)f2bf(e[2]) | ((unsigned)f2bf(e[3]) << 16)};
    *reinterpret_cast<u32x2*>(lds + L_P + (16 * ht + r16) * PROW + (16 * wave + 4 * g) * 2) = pk;
  }
#pragma unroll
  for (int s = 0; s < 12; ++s) *reinterpret_cast<u32x4*>(lds + L_BIG + (16 * wave + r16) * CROW + (32 * s + 8 * g) * 2) = cf[s];
  __syncthreads();
  if (tid < 32) {
    float M = -INFINITY, L = 0.f;
#pragma unroll
    for (int w8 = 0; w8 < 8; ++w8) { M = fmaxf(M, wmax[w8 * 32 + tid]); L += wsum[w8 * 32 + tid]; }
    st8_dev(p.ws, WS_BSTAT + (bb * H + tid) * 8, M, L);
  }
  {
    s16x8 pf[2][4];
#pragma unroll
    for (int ht = 0; ht < 2; ++ht)
#pragma unroll
      for (int ks = 0; ks < 4; ++ks) pf[ht][ks] = *reinterpret_cast<const s16x8*>(lds + L_P + (16 * ht + r16) * PROW + (32 * ks + 8 * g) * 2);
    const int q4 = r16 >> 2, p4 = r16 & 3;
#pragma unroll
    for (int ch = 0; ch < 2; ++ch) {
      if (ch == 1) {
        __syncthreads();
#pragma unroll
        for (int s = 0; s < 12; ++s) *reinterpret_cast<u32x4*>(lds + L_BIG + (16 * wave + r16) * CROW + (32 * s + 8 * g) * 2) = cf[12 + s];
        __syncthreads();
      }
#pragma unroll
      for (int ct = 0; ct < 3; ++ct) {
        f32x4 d[2] = {f32x4{0.f, 0.f, 0.f, 0.f}, f32x4{0.f, 0.f, 0.f, 0.f}};
#pragma unroll
        for (int ks = 0; ks < 4; ++ks) {
          const unsigned char* a0 = lds + L_BIG + (32 * ks + 8 * g + q4) * CROW + (48 * wave + 16 * ct + 4 * p4) * 2;
          typedef short v4i16_t __attribute__((ext_vector_type(4)));
          const v4i16_t t0 = __builtin_amdgcn_ds_read_tr16_b64_v4i16((LAS v4i16_t*)(a0));
          const v4i16_t t1 = __builtin_amdgcn_ds_read_tr16_b64_v4i16((LAS v4i16_t*)(a0 + 4 * CROW));
          const s16x8 af = {t0[0], t0[1], t0[2], t0[3], t1[0], t1[1], t1[2], t1[3]};
#pragma unroll
          for (int ht = 0; ht < 2; ++ht) d[ht] = __builtin_amdgcn_mfma_f32_16x16x32_bf16(af, pf[ht][ks], d[ht], 0, 0, 0);
        }
#pragma unroll
        for (int ht = 0; ht < 2; ++ht)
          st16_dev(rs, WS_BCTX + (unsigned)(((bb * H + 16 * ht + r16) * DC + 384 * ch + 48 * wave + 16 * ct + 4 * g) * 4), d[ht]);
      }
    }
  }
  publish(p.ws, WS_F2, bb, T);
  if (wave >= 6) {
    const u16* wp = p.Bwo + (size_t)(h * DH + 96 * half) * DM;
    const int wv6 = __builtin_amdgcn_readfirstlane(wave) - 6;
#pragma unroll 4
    for (int i = 0; i < 60; ++i)
      __builtin_amdgcn_global_load_lds((const GAS void*)(wp + ((wv6 * 60 + i) * 64 + lane) * 8), (LAS void*)(lds + LB_WO + (wv6 * 60 + i) * 1024), 16, 0, 2);
  }
  if (wave < 6) flag_wait(p.ws, WS_FZ, T, flag_load(p.ws, WS_FZ));
  wait_flags(p.ws, WS_F2, T);
  {
    float* coef = reinterpret_cast<float*>(lds + LB_COEF);
    float* part = reinterpret_cast<float*>(lds + LB_PART);
    float* red = reinterpret_cast<float*>(lds + LB_RED);
    float* cs = reinterpret_cast<float*>(lds + LB_CS);
    const int jg = tid / 96, q = tid % 96;
    f32x4 v[16];
    if (tid < 384) {
#pragma unroll
      for (int j = 0; j < 16; ++j) v[j] = ld16_dev(rs, WS_BCTX + (unsigned)((((16 * jg + j) * H + h) * DC + 384 * half + 4 * q) * 4));
    }
    if (tid < 64) {
      const f32x2 ml = ld8_dev(p.ws, WS_BSTAT + (tid * H + h) * 8);
      float M = ml[0];
#pragma unroll
      for (int o = 1; o < 64; o <<= 1) M = fmaxf(M, __shfl_xor(M, o));
      const float wt = __builtin_amdgcn_exp2f((ml[0] - M) * LOG2E);
      float L = wt * ml[1];
#pragma unroll
      for (int o = 1; o < 64; o <<= 1) L += __shfl_xor(L, o);
      coef[tid] = wt / L;
    }
    __syncthreads();
    if (tid < 384) {
      f32x4 a = {0.f, 0.f, 0.f, 0.f};
#pragma unroll
      for (int j = 0; j < 16; ++j) a += coef[16 * jg + j] * v[j];
      *reinterpret_cast<f32x4*>(part + jg * 384 + 4 * q) = a;
    }
    __syncthreads();
    float acc[24];
#pragma unroll
    for (int k = 0; k < 24; ++k) acc[k] = 0.f;
#pragma unroll
    for (int t = 0; t < 6; ++t) { const int row = 48 * wave + 8 * t + r; fma_row(acc, (part[row] + part[384 + row]) + (part[768 + row] + part[1152 + row]), wv[t]); }
#pragma unroll
    for (int k = 0; k < 24; ++k) { float x = acc[k]; x += dpp<ROR8>(x); x += __shfl_xor(x, 16); x += __shfl_xor(x, 32); acc[k] = x; }
    if (r == 0) {
#pragma unroll
      for (int i = 0; i < 3; ++i) {
        *reinterpret_cast<f32x4*>(red + wave * 192 + i * 64 + c * 8) = f32x4{acc[i * 8], acc[i * 8 + 1], acc[i * 8 + 2], acc[i * 8 + 3]};
        *reinterpret_cast<f32x4*>(red + wave * 192 + i * 64 + c * 8 + 4) = f32x4{acc[i * 8 + 4], acc[i * 8 + 5], acc[i * 8 + 6], acc[i * 8 + 7]};
      }
    }
    __syncthreads();
    if (tid < 192) {
      float s = 0.f;
#pragma unroll
      for (int w8 = 0; w8 < 8; ++w8) s += red[w8 * 192 + tid];
      cs[tid] = s;
      st_granule(p.ws, WS_XG + ((h * 2 + half) * DH + tid) * 8, granule(s));
    }
    float other = 0.f;
    if (tid < 128) {
      const unsigned off = WS_XG + ((h * 2 + (half ^ 1)) * DH + 96 * half + (tid < 96 ? tid : 0)) * 8;
      unsigned long long a = 0;
      for (unsigned spins = 0; spins < (1u << 22); ++spins) {
        a = ld_granule(p.ws, off);
        if (__all((a >> 32) == 1ull)) break;
        __builtin_amdgcn_s_sleep(1);
      }
      other = __builtin_bit_cast(float, (unsigned)a);
    }
    asm volatile("s_waitcnt vmcnt(0)" ::: "memory");
    __syncthreads();
    if (tid < 96) { const float own = cs[96 * half + tid]; cs[192 + tid] = bf16r(half ? other + own : own + other); }
    __syncthreads();
    float* red2 = reinterpret_cast<float*>(lds + LB_RED2);
    const int chunk = tid % 80, rg = tid / 80;
    if (tid < 480) {
      float a[8];
#pragma unroll
      for (int k = 0; k < 8; ++k) a[k] = 0.f;
#pragma unroll
      for (int i = 0; i < 16; ++i) {
        const float s = cs[192 + rg * 16 + i];
        const u32x4 wo = *reinterpret_cast<const u32x4*>(lds + LB_WO + (rg * 16 + i) * 1280 + chunk * 16);
#pragma unroll
        for (int k = 0; k < 4; ++k) { a[2 * k] = fmaf(s, bf_lo(wo[k]), a[2 * k]); a[2 * k + 1] = fmaf(s, bf_hi(wo[k]), a[2 * k + 1]); }
      }
      *reinterpret_cast<f32x4*>(red2 + rg * 640 + chunk * 8) = f32x4{a[0], a[1], a[2], a[3]};
      *reinterpret_cast<f32x4*>(red2 + rg * 640 + chunk * 8 + 4) = f32x4{a[4], a[5], a[6], a[7]};
    }
    __syncthreads();
    if (tid < 160) {
      f32x4 sum = *reinterpret_cast<const f32x4*>(red2 + tid * 4);
#pragma unroll
      for (int g = 1; g < 6; ++g) sum += *reinterpret_cast<const f32x4*>(red2 + g * 640 + tid * 4);
      const unsigned off = WS_OGB + (((tid >> 3) * NB + bb) * 32 + (tid & 7) * 4) * 8;
      st16_dev_u(rs, off, u32x4{f2u(sum[0]), 1u, f2u(sum[1]), 1u});
      st16_dev_u(rs, off + 16, u32x4{f2u(sum[2]), 1u, f2u(sum[3]), 1u});
    }
  }
  if (bb < 40) {
    float* buf = reinterpret_cast<float*>(lds + L_BIG);
    float* red = reinterpret_cast<float*>(lds + LB_RED2);
    const int np = bb < 20 ? 6 : 2;
    const unsigned gbase = bb < 20 ? WS_OG + bb * NA * 256 : WS_OGB + (bb - 20) * NB * 256;
    if (wave >= 6) flag_wait(p.ws, WS_FZ, T, flag_load(p.ws, WS_FZ));
    __syncthreads();
    {
      const int j = tid >> 4, q = tid & 15;
      u32x4 v[6];
      for (unsigned spins = 0; spins < (1u << 22); ++spins) {
        bool ok = true;
#pragma unroll
        for (int i = 0; i < 6; ++i) { v[i] = ld16_dev_u(rs, gbase + ((j + 32 * (i < np ? i : 0)) * 32 + 2 * q) * 8); ok = ok && v[i][1] == 1u && v[i][3] == 1u; }
        if (__all(ok)) break;
        __builtin_amdgcn_s_sleep(4);
      }
#pragma unroll
      for (int i = 0; i < 6; ++i) { buf[(j + 32 * i) * 32 + 2 * q] = i < np ? u2f(v[i][0]) : 0.f; buf[(j + 32 * i) * 32 + 2 * q + 1] = i < np ? u2f(v[i][2]) : 0.f; }
    }
    __syncthreads();
    if (tid < 256) {
      const int part = tid >> 5, col = tid & 31; float s = 0.f;
#pragma unroll
      for (int j = 0; j < 24; ++j) s += buf[(part * 24 + j) * 32 + col];
      red[tid] = s;
    }
    __syncthreads();
    if (tid < 32) {
      float s = 0.f;
#pragma unroll
      for (int j = 0; j < 8; ++j) s += red[j * 32 + tid];
      p.out[32 * bb + tid] = f2bf(s);
    }
    if (bb == 0 && tid == 0) {
      GAS u64* e = (GAS u64*)(p.ws + WS_EPOCH);
      __hip_atomic_store(e, __hip_atomic_load(e, __ATOMIC_RELAXED, __HIP_MEMORY_SCOPE_AGENT) + 1ull, __ATOMIC_RELAXED, __HIP_MEMORY_SCOPE_AGENT);
    }
  }
}

__global__ void __launch_bounds__(NTHR, 2) fused_decode_kernel(Params p) {
  __shared__ __attribute__((aligned(16))) unsigned char lds[LDS_BYTES];
  const int b = blockIdx.x;
  if (b < NA) {
    const int x = b & 7, idx = b >> 3;
    const int h = x * 4 + idx / ASPLIT, split = idx % ASPLIT;
    a_role(p, lds, h * ASPLIT + split, h, split);
  } else {
    const int bb = b - NA;
    b_role(p, lds, bb, (bb & 7) * 4 + (bb >> 4), (bb >> 3) & 1);
  }
}

extern "C" void kernel_launch(void* const* d_in, const int* in_sizes, int n_in,
                              void* d_out, int out_size, void* d_ws, size_t ws_size,
                              hipStream_t stream) {
  Params p;
  p.Ax = (const u16*)d_in[0]; p.Ak = (const u16*)d_in[1]; p.Av = (const u16*)d_in[2]; p.Awq = (const u16*)d_in[3]; p.Awo = (const u16*)d_in[4];
  p.Bx = (const u16*)d_in[5]; p.Bc = (const u16*)d_in[6]; p.Bwq = (const u16*)d_in[7]; p.Bwuk = (const u16*)d_in[8]; p.Bwuv = (const u16*)d_in[9]; p.Bwo = (const u16*)d_in[10];
  p.out = (u16*)d_out; p.ws = (unsigned char*)d_ws;
  fused_decode_kernel<<<NA + NB, NTHR, 0, stream>>>(p);
}
```
